```python
import torch

def _softmax(x, axis=-1):
    return torch.softmax(x, dim=axis)

def _gelu(x):
    return torch.nn.functional.gelu(x, approximate='tanh')

def _silu(x):
    return torch.nn.functional.silu(x)

def _sigmoid(x):
    return torch.sigmoid(x)

def _layernorm(x, axis=-1, eps=1e-05):
    m = x.mean(dim=axis, keepdim=True)
    v = x.var(dim=axis, keepdim=True, unbiased=False)
    return (x - m) / torch.sqrt(v + eps)

def _scatter_add(tgt, idx, src, axis=0):
    return tgt.clone().index_add_(axis, idx, src)

def _segment_sum(src, seg, n):
    out = torch.zeros((n,) + src.shape[1:], dtype=src.dtype)
    return out.index_add_(0, seg, src)

def _pad(x, pw):
    flat = tuple((v for pair in reversed(pw) for v in pair))
    return torch.nn.functional.pad(x, flat)
kernel_name = 'standalone__conformer_conv_module__speech'

def setup_inputs():

    _w = lambda a: (lambda t: t.to(torch.bfloat16) if t.is_floating_point() else t)(torch.as_tensor(a))
    B, T, D, K = (2, 4000, 512, 15)
    return {'x': _w((lambda *s: torch.randn(*s, dtype=torch.bfloat16) * 0.2)(B, T, D)), 'w_pw1': _w((lambda *s: torch.randn(*s, dtype=torch.bfloat16) * 0.2)(D, 2 * D)), 'dw': _w((lambda *s: torch.randn(*s, dtype=torch.bfloat16) * 0.2)(K, D)), 'w_pw2': _w((lambda *s: torch.randn(*s, dtype=torch.bfloat16) * 0.05)(D, D))}

def reference(x, w_pw1, dw, w_pw2):
    B, T, D = x.shape
    K = dw.shape[0]
    h = x @ w_pw1
    a, b = (h[..., :D].clone(), h[..., D:].clone())
    h = a * _sigmoid(b)
    pad = K // 2
    hp = _pad(h, ((0, 0), (pad, pad), (0, 0)))
    out = torch.zeros_like(h)
    for i in range(K):
        out = out + hp[:, i:i + T, :] * dw[i][None, None, :]
    return _silu(out) @ w_pw2
```

```cpp
#include <hip/hip_runtime.h>
#include <hip/hip_bf16.h>

typedef unsigned u32;
typedef __attribute__((ext_vector_type(2))) unsigned u32x2;
typedef __attribute__((ext_vector_type(4))) unsigned u32x4;
typedef __attribute__((ext_vector_type(2))) __bf16 bf2;
typedef __attribute__((ext_vector_type(4))) __bf16 bf4;
typedef __attribute__((ext_vector_type(8))) __bf16 bf8;
typedef __attribute__((ext_vector_type(2))) float f32x2;
typedef __attribute__((ext_vector_type(4))) float f32x4;
typedef __attribute__((ext_vector_type(16))) float f32x16;
#define LAS __attribute__((address_space(3)))
#define DEVI __device__ __forceinline__

#define ROT 0

template <int N> DEVI void waitv() { asm volatile("s_waitcnt vmcnt(%0)" ::"n"(N) : "memory"); }
DEVI void waitl0() { asm volatile("s_waitcnt lgkmcnt(0)" ::: "memory"); }
DEVI void bar() { __builtin_amdgcn_s_barrier(); }
DEVI void sbar() { __builtin_amdgcn_sched_barrier(0); }

DEVI u32 pk2(float a, float b) {
  f32x2 v = {a, b};
  bf2 r = __builtin_convertvector(v, bf2);
  return __builtin_bit_cast(u32, r);
}
DEVI float sigm(float v) {
  float e = __builtin_amdgcn_exp2f(v * -1.4426950408889634f);
  return __builtin_amdgcn_rcpf(1.0f + e);
}
DEVI bf8 ldb128(const char* p) { return *(const bf8*)p; }
DEVI bf8 ldtr(char* p0, char* p1) {
  bf4 a = __builtin_amdgcn_ds_read_tr16_b64_v4bf16((LAS bf4*)p0);
  bf4 b = __builtin_amdgcn_ds_read_tr16_b64_v4bf16((LAS bf4*)p1);
  return __builtin_shufflevector(a, b, 0, 1, 2, 3, 4, 5, 6, 7);
}
DEVI void glds(__amdgpu_buffer_rsrc_t r, u32 dst, u32 voff, u32 soff) {
  asm volatile("s_mov_b32 m0, %0\n\ts_nop 0\n\tbuffer_load_dwordx4 %1, %2, %3 offen lds" ::"s"(dst), "v"(voff), "s"(r), "s"(soff) : "memory");
}

constexpr int SLOT = 51200;
constexpr int DWOFF = 3 * SLOT;
constexpr int HSTR = 304;
constexpr int LDS1 = 3 * SLOT + 8192;
constexpr int LDS2 = 163840;

struct Frags { bf8 x[9]; bf8 a, b; };

__global__ __launch_bounds__(512, 2) void k_phase1(const char* __restrict__ x, const char* __restrict__ w1,
                                                   const char* __restrict__ dw, char* __restrict__ g) {
  extern __shared__ __attribute__((aligned(16))) char shm[];
  const int tid = threadIdx.x, lane = tid & 63;
  const int w = __builtin_amdgcn_readfirstlane(tid >> 6);
  const int b = blockIdx.x, xcd = b & 7, idx = b >> 3, gidx = idx >> 2, mem = idx & 3;
  const int grp = xcd * 8 + gidx, batch = grp >> 5, tile = grp & 31;
  const int t0 = tile * 125;
  const int k0 = ROT ? ((2 * mem + gidx) & 7) : 0;

  const __amdgpu_buffer_rsrc_t rx = __builtin_amdgcn_make_buffer_rsrc((void*)(x + (size_t)batch * 4096000), 0, 4096000, 0x00020000);
  const __amdgpu_buffer_rsrc_t rw = __builtin_amdgcn_make_buffer_rsrc((void*)w1, 0, 1048576, 0x00020000);
  const __amdgpu_buffer_rsrc_t rd = __builtin_amdgcn_make_buffer_rsrc((void*)dw, 0, 15360, 0x00020000);

  const u32 xoff0 = (u32)((t0 - 8 + 8 * w + (lane >> 3)) * 1024 + (((lane & 7) ^ ((4 * w + (lane >> 4)) & 7)) << 4));
  const u32 woff0 = (u32)((4 * w + (lane >> 4)) * 2048 + mem * 256 + (((lane & 15) ^ (((lane >> 4) << 2) | (w & 3))) << 4));

  const u32 lb = (u32)(size_t)(LAS char*)shm + w * 1024;
  auto piece = [&](int s, int i) {
    const int kc = (k0 + s) & 7;
    const u32 dst = lb + (s % 3) * SLOT;
    if (i == 0) glds(rx, dst, xoff0 + kc * 128, 0);
    if (i == 1) glds(rw, dst + 18432, woff0, kc * 131072);
    if (i == 2) glds(rw, dst + 34816, woff0, kc * 131072 + 1024);
    if (i == 3) glds(rx, dst + 8192, xoff0 + kc * 128 + 65536, 0);
    if (i == 4) glds(rw, dst + 18432 + 8192, woff0, kc * 131072 + 65536);
    if (i == 5) glds(rw, dst + 34816 + 8192, woff0, kc * 131072 + 65536 + 1024);
    if (i == 6) { if (w < 2) glds(rx, dst + 16384, xoff0 + kc * 128 + 131072, 0); }
  };
  auto stage = [&](int s) {
#pragma unroll
    for (int i = 0; i < 7; ++i) piece(s, i);
  };

  glds(rd, lb + DWOFF, (u32)((lane >> 1) * 1024 + mem * 256 + w * 32 + (lane & 1) * 16), 0);
  stage(0);
  stage(1);
  stage(2);

  const int l15 = lane & 15, q = lane >> 4;
  u32x4 T[8];
  {
    waitv<18>();
    u32x4 v[8];
#pragma unroll
    for (int j = 0; j < 8; ++j)
      v[j] = *(const u32x4*)(shm + DWOFF + w * 1024 + (((8 * q + j - (l15 & 7) - 1) & 31) << 5) + ((l15 >> 3) << 4));
#pragma unroll
    for (int c = 0; c < 8; ++c)
#pragma unroll
      for (int m = 0; m < 4; ++m)
        T[c][m] = __builtin_amdgcn_perm(v[2 * m + 1][c >> 1], v[2 * m][c >> 1], (c & 1) ? 0x07060302u : 0x05040100u);
  }

  const int xb0 = l15 * 128 + ((q ^ (l15 >> 1)) << 4);
  const int qq = l15 >> 2, p = lane & 3;
  const int wb0 = 18432 + 256 * (8 * q + qq) + (((2 * w + (p >> 1)) ^ ((qq << 2) | ((2 * q) & 3))) << 4) + 8 * (p & 1);
  const int wb1 = 18432 + 256 * (8 * q + 4 + qq) + (((2 * w + (p >> 1)) ^ ((qq << 2) | ((2 * q + 1) & 3))) << 4) + 8 * (p & 1);

  auto loadf = [&](Frags& f, int s, int ks) {
    char* base = shm + (s % 3) * SLOT;
    f.a = ldtr(base + wb0 + ks * 8192, base + wb1 + ks * 8192);
    f.b = ldtr(base + wb0 + ks * 8192 + 16384, base + wb1 + ks * 8192 + 16384);
#pragma unroll
    for (int m = 0; m < 9; ++m) f.x[m] = ldb128(base + (xb0 ^ (ks * 64)) + m * 2048);
  };

  f32x4 acca[9], accb[9];
#pragma unroll
  for (int m = 0; m < 9; ++m) { acca[m] = f32x4{0, 0, 0, 0}; accb[m] = f32x4{0, 0, 0, 0}; }
  auto mma = [&](const Frags& f, int snext) {
#pragma unroll
    for (int m = 0; m < 9; ++m) {
      acca[m] = __builtin_amdgcn_mfma_f32_16x16x32_bf16(f.x[m], f.a, acca[m], 0, 0, 0);
      accb[m] = __builtin_amdgcn_mfma_f32_16x16x32_bf16(f.x[m], f.b, accb[m], 0, 0, 0);
      if (snext < 8 && m < 7) { sbar(); piece(snext, m); sbar(); }
    }
  };

  Frags f0, f1;
  waitv<12>();
  bar();
  loadf(f0, 0, 0);
#pragma unroll
  for (int s = 0; s < 8; ++s) {
    sbar();
    loadf(f1, s, 1);
    sbar();
    mma(f0, 8);
    sbar();
    if (s < 7) {
      waitl0();
      if (s < 6) waitv<6>(); else waitv<0>();
      bar();
      loadf(f0, s + 1, 0);
      sbar();
    }
    mma(f1, s + 3);
  }

  char* him = shm + 2 * SLOT + w * (16 * HSTR);
#pragma unroll
  for (int m = 0; m < 9; ++m) {
    float h0 = acca[m][0] * sigm(accb[m][0]), h1 = acca[m][1] * sigm(accb[m][1]);
    float h2 = acca[m][2] * sigm(accb[m][2]), h3 = acca[m][3] * sigm(accb[m][3]);
    *(u32x2*)(him + l15 * HSTR + m * 32 + q * 8) = u32x2{pk2(h0, h1), pk2(h2, h3)};
  }
  const int hoff = ((q == 3 ? 0 : q) + l15) * 16;
  float sv[8][4];
#pragma unroll
  for (int c = 0; c < 8; ++c) {
    bf8 w0 = ldb128(him + c * HSTR + hoff), w1v = ldb128(him + (c + 8) * HSTR + hoff);
    bf8 t = __builtin_bit_cast(bf8, T[c]);
    f32x4 d0 = __builtin_amdgcn_mfma_f32_16x16x32_bf16(t, w0, f32x4{0, 0, 0, 0}, 0, 0, 0);
    f32x4 d1 = __builtin_amdgcn_mfma_f32_16x16x32_bf16(t, w1v, f32x4{0, 0, 0, 0}, 0, 0, 0);
#pragma unroll
    for (int r = 0; r < 4; ++r) {
      float v = lane < 32 ? d0[r] : d1[r];
      sv[c][r] = v * sigm(v);
    }
  }
  char* gb = g + (size_t)grp * 131072 + (mem * 8 + w) * 1024 + lane * 16;
#pragma unroll
  for (int r = 0; r < 4; ++r)
    *(u32x4*)(gb + r * 32768) = u32x4{pk2(sv[0][r], sv[1][r]), pk2(sv[2][r], sv[3][r]), pk2(sv[4][r], sv[5][r]), pk2(sv[6][r], sv[7][r])};
}

__global__ __launch_bounds__(256, 1) void k_phase2(const char* __restrict__ g, const char* __restrict__ w2, char* __restrict__ out) {
  extern __shared__ __attribute__((aligned(16))) char shm[];
  const int tid = threadIdx.x, lane = tid & 63;
  const int w = __builtin_amdgcn_readfirstlane(tid >> 6);
  const int b = blockIdx.x, xcd = b & 7, idx = b >> 3, gidx = idx >> 2, mem = idx & 3;
  const int grp = xcd * 8 + gidx, batch = grp >> 5, tile = grp & 31;
  const int t0 = tile * 125;
  const __amdgpu_buffer_rsrc_t rw = __builtin_amdgcn_make_buffer_rsrc((void*)w2, 0, 524288, 0x00020000);
  const u32 woff0 = (u32)((4 * w + (lane >> 4)) * 1024 + mem * 256 + (((lane & 15) ^ (((lane >> 4) << 2) | w)) << 4));
  const char* gb = g + (size_t)grp * 131072 + w * 32768;
  const u32 goff = lane * 16;
  const u32 lb = (u32)(size_t)(LAS char*)shm + w * 1024;
  u32x4 gf[32];
#pragma unroll
  for (int c = 0; c < 8; ++c) {
#pragma unroll
    for (int i = 0; i < 4; ++i) glds(rw, lb + c * 16384 + i * 4096, woff0, c * 65536 + i * 16384);
    const char* gc = gb + c * 4096;
    asm volatile("global_load_dwordx4 %0, %4, %5\n\tglobal_load_dwordx4 %1, %4, %5 offset:1024\n\t"
                 "global_load_dwordx4 %2, %4, %5 offset:2048\n\tglobal_load_dwordx4 %3, %4, %5 offset:3072"
                 : "=&v"(gf[4 * c]), "=&v"(gf[4 * c + 1]), "=&v"(gf[4 * c + 2]), "=&v"(gf[4 * c + 3])
                 : "v"(goff), "s"(gc) : "memory");
  }
  const int h = lane >> 5, blk = (lane >> 4) & 1, qq = (lane & 15) >> 2, p = lane & 3;
  int tb[2];
#pragma unroll
  for (int t = 0; t < 2; ++t) tb[t] = 256 * (8 * h + 4 * t + qq) + (((2 * blk + (p >> 1)) ^ ((qq << 2) | ((2 * h + t) & 3))) << 4) + 8 * (p & 1);
  f32x16 acc[4];
#pragma unroll
  for (int c = 0; c < 4; ++c)
#pragma unroll
    for (int r = 0; r < 16; ++r) acc[c][r] = 0.f;
#pragma unroll
  for (int c = 0; c < 8; ++c) {
    switch (c) {
      case 0: waitv<56>(); break; case 1: waitv<48>(); break; case 2: waitv<40>(); break; case 3: waitv<32>(); break;
      case 4: waitv<24>(); break; case 5: waitv<16>(); break; case 6: waitv<8>(); break; default: waitv<0>(); break;
    }
    asm volatile("" : "+v"(gf[4 * c]), "+v"(gf[4 * c + 1]), "+v"(gf[4 * c + 2]), "+v"(gf[4 * c + 3]));
    bar();
#pragma unroll
    for (int ks = 0; ks < 4; ++ks) {
      bf8 bfr = __builtin_bit_cast(bf8, gf[4 * c + ks]);
#pragma unroll
      for (int n = 0; n < 4; ++n) {
        char* base = shm + c * 16384 + ks * 4096;
        bf8 a = ldtr(base + (tb[0] ^ (n * 64)), base + (tb[1] ^ (n * 64)));
        acc[n] = __builtin_amdgcn_mfma_f32_32x32x16_bf16(a, bfr, acc[n], 0, 0, 0);
      }
    }
  }
  char* ob = shm + 131072 + w * 8192;
  const int rho = lane & 31;
#pragma unroll
  for (int n = 0; n < 4; ++n)
#pragma unroll
    for (int a = 0; a < 4; ++a)
      *(u32x2*)(ob + rho * 256 + (((4 * n + a) ^ (rho & 15)) << 4) + 8 * (h ^ (rho >> 4))) =
          u32x2{pk2(acc[n][4 * a], acc[n][4 * a + 1]), pk2(acc[n][4 * a + 2], acc[n][4 * a + 3])};
  char* orow = out + ((size_t)batch * 4000 + t0) * 1024 + mem * 256 + (lane & 15) * 16;
#pragma unroll
  for (int i = 0; i < 8; ++i) {
    const int r = 4 * i + (lane >> 4);
    u32x4 v = *(const u32x4*)(ob + r * 256 + (((lane & 15) ^ (r & 15)) << 4));
    if (i >= 4) v = u32x4{v[2], v[3], v[0], v[1]};
    const int j = 8 * (r & 15) + 4 * (r >> 4) + w;
    if (j < 125) *(u32x4*)(orow + j * 1024) = v;
  }
}

extern "C" void kernel_launch(void* const* d_in, const int* in_sizes, int n_in, void* d_out, int out_size, void* d_ws,
                              size_t ws_size, hipStream_t stream) {
  (void)hipFuncSetAttribute((const void*)k_phase1, hipFuncAttributeMaxDynamicSharedMemorySize, LDS1);
  (void)hipFuncSetAttribute((const void*)k_phase2, hipFuncAttributeMaxDynamicSharedMemorySize, LDS2);
  k_phase1<<<256, 512, LDS1, stream>>>((const char*)d_in[0], (const char*)d_in[1], (const char*)d_in[2], (char*)d_ws);
  k_phase2<<<256, 256, LDS2, stream>>>((const char*)d_ws, (const char*)d_in[3], (char*)d_out);
}
```

```cpp
#include <hip/hip_runtime.h>
#include <hip/hip_bf16.h>

typedef unsigned u32;
typedef unsigned long long u64;
typedef __attribute__((ext_vector_type(2))) unsigned u32x2;
typedef __attribute__((ext_vector_type(4))) unsigned u32x4;
typedef __attribute__((ext_vector_type(2))) __bf16 bf2;
typedef __attribute__((ext_vector_type(4))) __bf16 bf4;
typedef __attribute__((ext_vector_type(8))) __bf16 bf8;
typedef __attribute__((ext_vector_type(2))) float f32x2;
typedef __attribute__((ext_vector_type(4))) float f32x4;
typedef __attribute__((ext_vector_type(16))) float f32x16;
#define LAS __attribute__((address_space(3)))
#define GAS __attribute__((address_space(1)))
#define DEVI __device__ __forceinline__
#define RLX_AGENT __ATOMIC_RELAXED, __HIP_MEMORY_SCOPE_AGENT

#define ROT 0

template <int N> DEVI void waitv() { asm volatile("s_waitcnt vmcnt(%0)" ::"n"(N) : "memory"); }
DEVI void waitl0() { asm volatile("s_waitcnt lgkmcnt(0)" ::: "memory"); }
DEVI void sbar() { __builtin_amdgcn_sched_barrier(0); }
DEVI void bar() { asm volatile("s_barrier" ::: "memory"); }

DEVI u32 pk2(float a, float b) {
  f32x2 v = {a, b};
  bf2 r = __builtin_convertvector(v, bf2);
  return __builtin_bit_cast(u32, r);
}
DEVI float sigm(float v) {
  float e = __builtin_amdgcn_exp2f(v * -1.4426950408889634f);
  return __builtin_amdgcn_rcpf(1.0f + e);
}
DEVI bf8 ldb128(const char* p) { return *(const bf8*)p; }
DEVI bf8 ldtr(char* p0, char* p1) {
  bf4 a = __builtin_amdgcn_ds_read_tr16_b64_v4bf16((LAS bf4*)p0);
  bf4 b = __builtin_amdgcn_ds_read_tr16_b64_v4bf16((LAS bf4*)p1);
  return __builtin_shufflevector(a, b, 0, 1, 2, 3, 4, 5, 6, 7);
}
DEVI void glds(__amdgpu_buffer_rsrc_t r, u32 dst, u32 voff, u32 soff) {
  asm volatile("s_mov_b32 m0, %0\n\ts_nop 0\n\tbuffer_load_dwordx4 %1, %2, %3 offen lds" ::"s"(dst), "v"(voff), "s"(r), "s"(soff) : "memory");
}

constexpr int WS = 16384;
constexpr int XS = 18432;
constexpr int XOFF = 6 * WS;
constexpr int SPARE = XOFF + 3 * XS;
constexpr int DWOFF = SPARE;
constexpr int DUMMY = SPARE + 4096;
constexpr int CTL = SPARE + 5120;
constexpr int LDSZ = 163840;
constexpr int HSTR = 288;
constexpr size_t FLAGOFF = 16u << 20;

#define STAMP(i)
#define STAMP_INIT
#define STAMP_OUT

__global__ __launch_bounds__(512, 2) void k_conv_module(const char* __restrict__ x, const char* __restrict__ w1,
                                                        const char* __restrict__ dw, const char* __restrict__ w2,
                                                        char* __restrict__ ws, char* __restrict__ out) {
  extern __shared__ __attribute__((aligned(16))) char shm[];
  const int tid = threadIdx.x, lane = tid & 63;
  const int w = __builtin_amdgcn_readfirstlane(tid >> 6);
  STAMP_INIT;
  STAMP(0);
  const int u = w & 3;
  const int b = blockIdx.x, xcd = b & 7, idx = b >> 3, gidx = idx >> 2, mem = idx & 3;
  const int grp = xcd * 8 + gidx, batch = grp >> 5, tile = grp & 31;
  const int t0 = tile * 125;
  const int k0 = ROT ? ((2 * mem + gidx) & 7) : 0;
  const int l15 = lane & 15, q = lane >> 4;
  const u32 lb = (u32)(size_t)(LAS char*)shm + u * 1024;
  const __amdgpu_buffer_rsrc_t rv = __builtin_amdgcn_make_buffer_rsrc((void*)w2, 0, 524288, 0x00020000);
  const u32 voff0 = (u32)((4 * u + q) * 1024 + mem * 256 + ((l15 ^ ((q << 2) | u)) << 4));
  auto vunit = [&](int n, u32 slot) {
    const u32 c = 2 * ((mem + n) & 3) + (n >> 2);
#pragma unroll
    for (int i = 0; i < 4; ++i) glds(rv, lb + slot + i * 4096, voff0, c * 65536 + i * 16384);
  };
  GAS u64* flags = (GAS u64*)(ws + FLAGOFF + grp * 256);
  char* gbase = ws + (size_t)grp * 131072;

  if (w >= 4) {
    const __amdgpu_buffer_rsrc_t rx = __builtin_amdgcn_make_buffer_rsrc((void*)(x + (size_t)batch * 4096000), 0, 4096000, 0x00020000);
    const __amdgpu_buffer_rsrc_t rw = __builtin_amdgcn_make_buffer_rsrc((void*)w1, 0, 1048576, 0x00020000);
    const __amdgpu_buffer_rsrc_t rd = __builtin_amdgcn_make_buffer_rsrc((void*)dw, 0, 15360, 0x00020000);
    u32 xo[5];
    xo[0] = (u32)((t0 - 8 + 8 * u + (lane >> 3)) * 1024 + (((lane & 7) ^ ((4 * u + q) & 7)) << 4));
#pragma unroll
    for (int i = 1; i < 5; ++i) xo[i] = xo[0] + i * 32768;
    if (u >= 2) xo[4] = 0x80000000u;
    const u32 x4d = u < 2 ? 16384u : 0u;
    const u32 woff0 = (u32)((4 * u + q) * 2048 + mem * 256 + ((l15 ^ ((q << 2) | u)) << 4));
    auto xunit = [&](int P) {
      const u32 so = ((k0 + P) & 7) * 128;
      const u32 d = lb + XOFF + (P % 3) * XS;
#pragma unroll
      for (int i = 0; i < 4; ++i) glds(rx, d + i * 4096, xo[i], so);
      glds(rx, u < 2 ? d + x4d : (u32)(size_t)(LAS char*)shm + DUMMY, xo[4], so);
    };
    auto wunit = [&](int j) {
      const u32 so = (((k0 + (j >> 1)) & 7) * 64 + (j & 1) * 32) * 2048;
      const u32 d = lb + (j % 6) * WS;
      glds(rw, d, woff0, so);
      glds(rw, d + 8192, woff0, so + 1024);
      glds(rw, d + 4096, woff0, so + 32768);
      glds(rw, d + 12288, woff0, so + 32768 + 1024);
    };
    glds(rd, lb + DWOFF, (u32)((lane >> 2) * 1024 + mem * 256 + ((lane >> 1) & 1) * 128 + u * 32 + (lane & 1) * 16), 0);
    xunit(0); wunit(0); wunit(1);
    waitv<4>(); bar();
    STAMP(1);
    xunit(1); wunit(2); waitv<9>(); bar();
    wunit(3); xunit(2); waitv<9>(); bar();
    wunit(4); wunit(5); waitv<13>(); bar();
    xunit(3); wunit(6); waitv<13>(); bar();
    STAMP(2);
    wunit(7); wunit(8); waitv<17>(); bar();
    xunit(4); wunit(9); waitv<17>(); bar();
    wunit(10);
    u32x4 T[2][8];
#pragma unroll
    for (int blk = 0; blk < 2; ++blk) {
      u32x4 v[8];
#pragma unroll
      for (int j = 0; j < 8; ++j) {
        const int jj = j ^ ((l15 >> 3) << 2);
        const u32 i = (u32)(8 * q + jj - (l15 & 7) - 1);
        v[j] = *(const u32x4*)(shm + DWOFF + u * 1024 + (min(i, 15u) << 6) + blk * 32 + ((l15 >> 3) << 4));
      }
#pragma unroll
      for (int c = 0; c < 8; ++c)
#pragma unroll
        for (int m = 0; m < 4; ++m) {
          T[blk][c][m] = __builtin_amdgcn_perm(v[2 * m + 1][c >> 1], v[2 * m][c >> 1], (c & 1) ? 0x07060302u : 0x05040100u);
        }
#pragma unroll
      for (int c = 0; c < 8; ++c) asm volatile("" : "+v"(T[blk][c]));
    }
    waitv<17>(); bar();
    xunit(5); wunit(11); waitv<17>(); bar();
    STAMP(3);
    wunit(12); waitv<17>(); bar();
    xunit(6); wunit(13); waitv<17>(); bar();
    wunit(14); waitv<17>(); bar();
    xunit(7); wunit(15); waitv<17>(); bar();
    STAMP(4);
    vunit(0, 4 * WS); waitv<17>(); bar();
    vunit(1, 5 * WS); waitv<12>(); bar();
    vunit(2, 0 * WS); waitv<12>(); bar();
    vunit(3, 1 * WS); bar();
    STAMP(5);
    vunit(4, 2 * WS);
    const u64 tag = *(LAS u64*)(shm + CTL);
    const int hoff = ((q == 3 ? 0 : q) + l15) * 16;
#pragma unroll 1
    for (int blk = 0; blk < 2; ++blk) {
      bar();
      STAMP(6 + 3 * blk);
      const char* him = shm + XOFF + (blk ? 0 : 2 * XS) + u * (16 * HSTR);
      float sv[8][4];
      bf8 wv[16];
#pragma unroll
      for (int c = 0; c < 16; ++c) wv[c] = ldb128(him + c * HSTR + (hoff ^ (((c >> 2) & 1) << 4)));
#pragma unroll
      for (int c = 0; c < 8; ++c) {
        bf8 t = __builtin_bit_cast(bf8, blk ? T[1][c] : T[0][c]);
        f32x4 d0 = __builtin_amdgcn_mfma_f32_16x16x32_bf16(t, wv[c], f32x4{0, 0, 0, 0}, 0, 0, 0);
        f32x4 d1 = __builtin_amdgcn_mfma_f32_16x16x32_bf16(t, wv[c + 8], f32x4{0, 0, 0, 0}, 0, 0, 0);
#pragma unroll
        for (int r = 0; r < 4; ++r) {
          float v = lane < 32 ? d0[r] : d1[r];
          sv[c][r] = v * sigm(v);
        }
      }
#pragma unroll
      for (int r = 0; r < 4; ++r) {
        const u32x4 d = u32x4{pk2(sv[0][r], sv[1][r]), pk2(sv[2][r], sv[3][r]), pk2(sv[4][r], sv[5][r]), pk2(sv[6][r], sv[7][r])};
        asm volatile("global_store_dwordx4 %0, %1, %2 sc1\n\ts_nop 1" ::"v"(lane * 16), "v"(d), "s"(gbase + (r * 32 + mem * 8 + blk * 4 + u) * 1024) : "memory");
      }
      STAMP(7 + 3 * blk);
      waitv<0>();
      if (lane == 0) {
        const u32 old = __hip_atomic_fetch_add(((LAS u32*)shm) + (CTL + 8) / 4 + blk, 1u, __ATOMIC_RELAXED, __HIP_MEMORY_SCOPE_WORKGROUP);
        if (old == 3) __hip_atomic_store(flags + mem * 2 + blk, tag, RLX_AGENT);
      }
      STAMP(8 + 3 * blk);
    }
  } else {
    if (w == 0 && lane == 0) {
      const u64 G = __hip_atomic_load(flags + 16, RLX_AGENT);
      u64 tg = (G ^ (G >> 29)) * 0x9E3779B97F4A7C15ull + 0x632BE59BD9B4E019ull;
      if (tg == G) ++tg;
      *(LAS u64*)(shm + CTL) = tg;
      *(LAS u64*)(shm + CTL + 8) = 0;
    }
    const int qq = l15 >> 2, p = lane & 3;
    const int xb = XOFF + l15 * 128 + ((q ^ (l15 >> 1)) << 4);
    int wb[2];
#pragma unroll
    for (int t = 0; t < 2; ++t) wb[t] = 256 * (8 * q + 4 * t + qq) + (((2 * u + (p >> 1)) ^ ((qq << 2) | ((2 * q + t) & 3))) << 4) + 8 * (p & 1);
    f32x4 acc[9][4];
#pragma unroll
    for (int m = 0; m < 9; ++m)
#pragma unroll
      for (int n = 0; n < 4; ++n) acc[m][n] = f32x4{0, 0, 0, 0};
    bf8 xf[9], wf[2][4];
    auto ldw = [&](int set, int n, int slot) {
      char* base = shm + slot + (n & 1) * 8192;
      wf[set][n] = ldtr(base + (wb[0] ^ ((n >> 1) * 128)), base + (wb[1] ^ ((n >> 1) * 128)));
    };
    waitl0();
    bar();
    STAMP(1);
#pragma unroll
    for (int n = 0; n < 4; ++n) ldw(0, n, 0);
#pragma unroll
    for (int m = 0; m < 4; ++m) xf[m] = ldb128(shm + xb + m * 2048);
    int xs = 0, wsn = WS;
#pragma unroll 1
    for (int P = 0; P < 8; ++P) {
      const int xsn = xs == 2 * XS ? 0 : xs + XS;
#pragma unroll
      for (int ks = 0; ks < 2; ++ks) {
        const char* xc = shm + ((xb + xs) ^ (ks * 64));
        const char* xn = ks == 0 ? shm + ((xb + xs) ^ 64) : shm + xb + xsn;
#pragma unroll
        for (int m = 0; m < 9; ++m) {
          sbar();
          if (m == 5) bar();
          if (m < 5) xf[m + 4] = ldb128(xc + (m + 4) * 2048);
          if (m >= 5) ldw(ks ^ 1, m - 5, wsn);
          const bf8 xv = xf[m];
          if (m >= 5) xf[m - 5] = ldb128(xn + (m - 5) * 2048);
          sbar();
#pragma unroll
          for (int n = 0; n < 4; ++n) acc[m][n] = __builtin_amdgcn_mfma_f32_16x16x32_bf16(xv, wf[ks][n], acc[m][n], 0, 0, 0);
        }
        wsn = wsn == 5 * WS ? 0 : wsn + WS;
      }
      xs = xsn;
    }
    sbar();
    STAMP(5);
#pragma unroll
    for (int blk = 0; blk < 2; ++blk) {
      char* him = shm + XOFF + (blk ? 0 : 2 * XS) + u * (16 * HSTR) + l15 * HSTR + ((((q >> 1) ^ ((l15 >> 2) & 1)) << 4) | (((q & 1) ^ (l15 >> 3)) << 3));
#pragma unroll
      for (int m = 0; m < 9; ++m) {
        const f32x4 a = acc[m][2 * blk], g = acc[m][2 * blk + 1];
        *(u32x2*)(him + m * 32) = u32x2{pk2(a[0] * sigm(g[0]), a[1] * sigm(g[1])), pk2(a[2] * sigm(g[2]), a[3] * sigm(g[3]))};
      }
      STAMP(6 + 3 * blk);
      sbar();
      waitl0();
      bar();
      sbar();
      STAMP(7 + 3 * blk);
      if (blk == 0) { vunit(5, 3 * WS); vunit(6, XOFF + XS); }
      else vunit(7, XOFF + 2 * XS);
    }
  }
  STAMP(12);
  const int role = w >> 2;
  const u64 tag = *(LAS u64*)(shm + CTL);
  {
    GAS u64* fp = flags + (lane & 3) * 2 + role;
    for (int it = 0; it < (1 << 20); ++it) {
      const u64 f = __hip_atomic_load(fp, RLX_AGENT);
      if (__all(f == tag)) break;
      __builtin_amdgcn_s_sleep(1);
    }
  }
  STAMP(13);
  if (w == 0 && mem == 0 && lane == 0) __hip_atomic_store(flags + 16, tag, RLX_AGENT);
  u32x4 gf[16];
  {
    const u32 goff = lane * 16;
#pragma unroll
    for (int n = 0; n < 4; ++n) {
      const char* gc = gbase + (u * 32 + ((mem + n) & 3) * 8 + role * 4) * 1024;
      asm volatile("global_load_dwordx4 %0, %4, %5 sc1\n\tglobal_load_dwordx4 %1, %4, %5 offset:1024 sc1\n\t"
                   "global_load_dwordx4 %2, %4, %5 offset:2048 sc1\n\tglobal_load_dwordx4 %3, %4, %5 offset:3072 sc1"
                   : "=&v"(gf[4 * n]), "=&v"(gf[4 * n + 1]), "=&v"(gf[4 * n + 2]), "=&v"(gf[4 * n + 3])
                   : "v"(goff), "s"(gc) : "memory");
    }
  }
  if (role) bar();
  const int h = lane >> 5, rho = lane & 31;
  int tb[2];
  {
    const int blk = q & 1, qq = l15 >> 2, p = lane & 3;
#pragma unroll
    for (int t = 0; t < 2; ++t) tb[t] = 256 * (8 * h + 4 * t + qq) + (((2 * blk + (p >> 1)) ^ ((qq << 2) | ((2 * h + t) & 3))) << 4) + 8 * (p & 1);
  }
  int vs[4];
  vs[0] = role ? 2 * WS : 4 * WS; vs[1] = role ? 3 * WS : 5 * WS; vs[2] = role ? XOFF + XS : 0; vs[3] = role ? XOFF + 2 * XS : WS;
  f32x16 acc2[4];
#pragma unroll
  for (int c = 0; c < 4; ++c)
#pragma unroll
    for (int r = 0; r < 16; ++r) acc2[c][r] = 0.f;
  bf8 af[2][4];
  auto lda = [&](int set, int step) {
    char* base = shm + vs[step >> 2] + (step & 3) * 4096;
#pragma unroll
    for (int n = 0; n < 4; ++n) af[set][n] = ldtr(base + (tb[0] ^ (n * 64)), base + (tb[1] ^ (n * 64)));
  };
  lda(0, 0);
#pragma unroll
  for (int step = 0; step < 16; ++step) {
    sbar();
    if (step < 15) lda((step + 1) & 1, step + 1);
    if (step == 0) { waitv<12>(); STAMP(14); }
    if (step == 4) waitv<8>();
    if (step == 8) waitv<4>();
    if (step == 12) waitv<0>();
    if ((step & 3) == 0) asm volatile("" : "+v"(gf[step]), "+v"(gf[step + 1]), "+v"(gf[step + 2]), "+v"(gf[step + 3]));
    sbar();
    const bf8 bfr = __builtin_bit_cast(bf8, gf[step]);
#pragma unroll
    for (int n = 0; n < 4; ++n) acc2[n] = __builtin_amdgcn_mfma_f32_32x32x16_bf16(af[step & 1][n], bfr, acc2[n], 0, 0, 0);
  }
  sbar();
  STAMP(15);
  char* pb = shm + (u < 2 ? (4 + u) * WS : (u - 2) * WS);
  if (!role) {
    bar();
#pragma unroll
    for (int n = 0; n < 4; ++n)
#pragma unroll
      for (int a = 0; a < 4; ++a)
        *(f32x4*)(pb + (n * 4 + a) * 1024 + lane * 16) = f32x4{acc2[n][4 * a], acc2[n][4 * a + 1], acc2[n][4 * a + 2], acc2[n][4 * a + 3]};
    waitl0();
    bar();
    STAMP(16);
    STAMP_OUT;
    return;
  }
  bar();
  STAMP(16);
  u32x2 o[16];
#pragma unroll
  for (int n = 0; n < 4; ++n)
#pragma unroll
    for (int a = 0; a < 4; ++a) {
      const f32x4 v = *(const f32x4*)(pb + (n * 4 + a) * 1024 + lane * 16);
      o[n * 4 + a] = u32x2{pk2(acc2[n][4 * a] + v[0], acc2[n][4 * a + 1] + v[1]), pk2(acc2[n][4 * a + 2] + v[2], acc2[n][4 * a + 3] + v[3])};
    }
  sbar();
#pragma unroll
  for (int i = 0; i < 16; ++i) *(u32x2*)(pb + rho * 256 + ((i ^ (rho & 15)) << 4) + 8 * (h ^ (rho >> 4))) = o[i];
  char* orow = out + ((size_t)batch * 4000 + t0) * 1024 + mem * 256 + l15 * 16;
#pragma unroll
  for (int i = 0; i < 8; ++i) {
    const int r = 4 * i + q;
    u32x4 v = *(const u32x4*)(pb + r * 256 + ((l15 ^ (r & 15)) << 4));
    if (i >= 4) v = u32x4{v[2], v[3], v[0], v[1]};
    const int j = 8 * (r & 15) + 4 * (r >> 4) + u;
    if (j < 125) *(u32x4*)(orow + j * 1024) = v;
  }
  STAMP(17);
  STAMP(18);
  STAMP_OUT;
}

extern "C" void kernel_launch(void* const* d_in, const int* in_sizes, int n_in, void* d_out, int out_size, void* d_ws,
                              size_t ws_size, hipStream_t stream) {
  (void)hipFuncSetAttribute((const void*)k_conv_module, hipFuncAttributeMaxDynamicSharedMemorySize, LDSZ);
  k_conv_module<<<256, 512, LDSZ, stream>>>((const char*)d_in[0], (const char*)d_in[1], (const char*)d_in[2], (const char*)d_in[3],
                                            (char*)d_ws, (char*)d_out);
}
```

```cpp
#include <hip/hip_runtime.h>
#include <hip/hip_bf16.h>

typedef unsigned u32;
typedef unsigned long long u64;
typedef __attribute__((ext_vector_type(2))) unsigned u32x2;
typedef __attribute__((ext_vector_type(4))) unsigned u32x4;
typedef __attribute__((ext_vector_type(2))) __bf16 bf2;
typedef __attribute__((ext_vector_type(4))) __bf16 bf4;
typedef __attribute__((ext_vector_type(8))) __bf16 bf8;
typedef __attribute__((ext_vector_type(2))) float f32x2;
typedef __attribute__((ext_vector_type(4))) float f32x4;
typedef __attribute__((ext_vector_type(16))) float f32x16;
#define LAS __attribute__((address_space(3)))
#define GAS __attribute__((address_space(1)))
#define DEVI __device__ __forceinline__
#define RLX_AGENT __ATOMIC_RELAXED, __HIP_MEMORY_SCOPE_AGENT

#define ROT 0

template <int N> DEVI void waitv() { asm volatile("s_waitcnt vmcnt(%0)" ::"n"(N) : "memory"); }
DEVI void waitl0() { asm volatile("s_waitcnt lgkmcnt(0)" ::: "memory"); }
DEVI void sbar() { __builtin_amdgcn_sched_barrier(0); }
DEVI void bar() { asm volatile("s_barrier" ::: "memory"); }

DEVI u32 pk2(float a, float b) {
  f32x2 v = {a, b};
  bf2 r = __builtin_convertvector(v, bf2);
  return __builtin_bit_cast(u32, r);
}
DEVI float sigm(float v) {
  float e = __builtin_amdgcn_exp2f(v * -1.4426950408889634f);
  return __builtin_amdgcn_rcpf(1.0f + e);
}
DEVI bf8 ldb128(const char* p) { return *(const bf8*)p; }
DEVI bf8 ldtr(char* p0, char* p1) {
  bf4 a = __builtin_amdgcn_ds_read_tr16_b64_v4bf16((LAS bf4*)p0);
  bf4 b = __builtin_amdgcn_ds_read_tr16_b64_v4bf16((LAS bf4*)p1);
  return __builtin_shufflevector(a, b, 0, 1, 2, 3, 4, 5, 6, 7);
}
DEVI void glds(__amdgpu_buffer_rsrc_t r, u32 dst, u32 voff, u32 soff) {
  asm volatile("s_mov_b32 m0, %0\n\ts_nop 0\n\tbuffer_load_dwordx4 %1, %2, %3 offen lds" ::"s"(dst), "v"(voff), "s"(r), "s"(soff) : "memory");
}

constexpr int WS = 16384;
constexpr int XS = 18432;
constexpr int XOFF = 6 * WS;
constexpr int SPARE = XOFF + 3 * XS;
constexpr int DWOFF = SPARE;
constexpr int DUMMY = SPARE + 4096;
constexpr int CTL = SPARE + 5120;
constexpr int LDSZ = 163840;
constexpr int HSTR = 288;
constexpr size_t FLAGOFF = 16u << 20;

DEVI u64 now() { u64 t; asm volatile("s_memrealtime %0\n\ts_waitcnt lgkmcnt(0)" : "=s"(t) :: "memory"); return t; }
#define PADAT -1
#define PADT 0
#define PAD(i) do { if (PADAT == (i)) { while (now() - tstart < (u64)(PADT)) __builtin_amdgcn_s_sleep(1); } } while (0)
#define STAMP(i)
#define STAMP_INIT
#define STAMP_OUT

__global__ __launch_bounds__(512, 2) void k_conv_module(const char* __restrict__ x, const char* __restrict__ w1,
                                                        const char* __restrict__ dw, const char* __restrict__ w2,
                                                        char* __restrict__ ws, char* __restrict__ out) {
  extern __shared__ __attribute__((aligned(16))) char shm[];
  const int tid = threadIdx.x, lane = tid & 63;
  const int w = __builtin_amdgcn_readfirstlane(tid >> 6);
  const u64 tstart = PADAT >= 0 ? now() : 0;
  STAMP_INIT;
  STAMP(0);
  const int u = w & 3;
  const int b = blockIdx.x, xcd = b & 7, idx = b >> 3, gidx = idx >> 2, mem = idx & 3;
  const int grp = xcd * 8 + gidx, batch = grp >> 5, tile = grp & 31;
  const int t0 = tile * 125;
  const int k0 = ROT ? ((2 * mem + gidx) & 7) : 0;
  const int l15 = lane & 15, q = lane >> 4;
  const u32 lb = (u32)(size_t)(LAS char*)shm + u * 1024;
  const __amdgpu_buffer_rsrc_t rv = __builtin_amdgcn_make_buffer_rsrc((void*)w2, 0, 524288, 0x00020000);
  const u32 voff0 = (u32)((4 * u + q) * 1024 + mem * 256 + ((l15 ^ ((q << 2) | u)) << 4));
  auto vunit = [&](int n, u32 slot) {
    const u32 c = 2 * ((mem + n) & 3) + (n >> 2);
#pragma unroll
    for (int i = 0; i < 4; ++i) glds(rv, lb + slot + i * 4096, voff0, c * 65536 + i * 16384);
  };
  GAS u64* flags = (GAS u64*)(ws + FLAGOFF + grp * 256);
  char* gbase = ws + (size_t)grp * 131072;

  if (w >= 4) {
    const __amdgpu_buffer_rsrc_t rx = __builtin_amdgcn_make_buffer_rsrc((void*)(x + (size_t)batch * 4096000), 0, 4096000, 0x00020000);
    const __amdgpu_buffer_rsrc_t rw = __builtin_amdgcn_make_buffer_rsrc((void*)w1, 0, 1048576, 0x00020000);
    const __amdgpu_buffer_rsrc_t rd = __builtin_amdgcn_make_buffer_rsrc((void*)dw, 0, 15360, 0x00020000);
    u32 xo[5];
    xo[0] = (u32)((t0 - 8 + 8 * u + (lane >> 3)) * 1024 + (((lane & 7) ^ ((4 * u + q) & 7)) << 4));
#pragma unroll
    for (int i = 1; i < 5; ++i) xo[i] = xo[0] + i * 32768;
    if (u >= 2) xo[4] = 0x80000000u;
    const u32 x4d = u < 2 ? 16384u : 0u;
    const u32 woff0 = (u32)((4 * u + q) * 2048 + mem * 256 + ((l15 ^ ((q << 2) | u)) << 4));
    auto xunit = [&](int P) {
      const u32 so = ((k0 + P) & 7) * 128;
      const u32 d = lb + XOFF + (P % 3) * XS;
#pragma unroll
      for (int i = 0; i < 4; ++i) glds(rx, d + i * 4096, xo[i], so);
      glds(rx, u < 2 ? d + x4d : (u32)(size_t)(LAS char*)shm + DUMMY, xo[4], so);
    };
    auto wunit = [&](int j) {
      const u32 so = (((k0 + (j >> 1)) & 7) * 64 + (j & 1) * 32) * 2048;
      const u32 d = lb + (j % 6) * WS;
      glds(rw, d, woff0, so);
      glds(rw, d + 8192, woff0, so + 1024);
      glds(rw, d + 4096, woff0, so + 32768);
      glds(rw, d + 12288, woff0, so + 32768 + 1024);
    };
    glds(rd, lb + DWOFF, (u32)((lane >> 2) * 1024 + mem * 256 + ((lane >> 1) & 1) * 128 + u * 32 + (lane & 1) * 16), 0);
    xunit(0); wunit(0); wunit(1);
    waitv<4>(); bar();
    STAMP(1);
    xunit(1); wunit(2); waitv<9>(); bar();
    wunit(3); xunit(2); waitv<9>(); bar();
    wunit(4); wunit(5); waitv<13>(); bar();
    xunit(3); wunit(6); waitv<13>(); bar();
    STAMP(2);
    wunit(7); wunit(8); waitv<17>(); bar();
    xunit(4); wunit(9); waitv<17>(); bar();
    wunit(10);
    u32x4 T[2][8];
#pragma unroll
    for (int blk = 0; blk < 2; ++blk) {
      u32x4 v[8];
#pragma unroll
      for (int j = 0; j < 8; ++j) {
        const int jj = j ^ ((l15 >> 3) << 2);
        const u32 i = (u32)(8 * q + jj - (l15 & 7) - 1);
        v[j] = *(const u32x4*)(shm + DWOFF + u * 1024 + (min(i, 15u) << 6) + blk * 32 + ((l15 >> 3) << 4));
      }
#pragma unroll
      for (int c = 0; c < 8; ++c)
#pragma unroll
        for (int m = 0; m < 4; ++m) {
          T[blk][c][m] = __builtin_amdgcn_perm(v[2 * m + 1][c >> 1], v[2 * m][c >> 1], (c & 1) ? 0x07060302u : 0x05040100u);
        }
#pragma unroll
      for (int c = 0; c < 8; ++c) asm volatile("" : "+v"(T[blk][c]));
    }
    waitv<17>(); bar();
    xunit(5); wunit(11); waitv<17>(); bar();
    STAMP(3);
    wunit(12); waitv<17>(); bar();
    xunit(6); wunit(13); waitv<17>(); bar();
    wunit(14); waitv<17>(); bar();
    xunit(7); wunit(15); waitv<17>(); bar();
    STAMP(4);
    vunit(0, 4 * WS); waitv<17>(); bar();
    vunit(1, 5 * WS); waitv<12>(); bar();
    vunit(2, 0 * WS); waitv<12>(); bar();
    vunit(3, 1 * WS); bar();
    STAMP(5);
    vunit(4, 2 * WS);
    const u64 tag = *(LAS u64*)(shm + CTL);
    const int hoff = ((q == 3 ? 0 : q) + l15) * 16;
#pragma unroll 1
    for (int blk = 0; blk < 2; ++blk) {
      bar();
      STAMP(6 + 3 * blk);
      const char* him = shm + XOFF + (blk ? 0 : 2 * XS) + u * (16 * HSTR);
      float sv[8][4];
      bf8 wv[16];
#pragma unroll
      for (int c = 0; c < 16; ++c) wv[c] = ldb128(him + c * HSTR + (hoff ^ (((c >> 2) & 1) << 4)));
#pragma unroll
      for (int c = 0; c < 8; ++c) {
        bf8 t = __builtin_bit_cast(bf8, blk ? T[1][c] : T[0][c]);
        f32x4 d0 = __builtin_amdgcn_mfma_f32_16x16x32_bf16(t, wv[c], f32x4{0, 0, 0, 0}, 0, 0, 0);
        f32x4 d1 = __builtin_amdgcn_mfma_f32_16x16x32_bf16(t, wv[c + 8], f32x4{0, 0, 0, 0}, 0, 0, 0);
#pragma unroll
        for (int r = 0; r < 4; ++r) {
          float v = lane < 32 ? d0[r] : d1[r];
          sv[c][r] = v * sigm(v);
        }
      }
#pragma unroll
      for (int r = 0; r < 4; ++r) {
        const u32x4 d = u32x4{pk2(sv[0][r], sv[1][r]), pk2(sv[2][r], sv[3][r]), pk2(sv[4][r], sv[5][r]), pk2(sv[6][r], sv[7][r])};
        asm volatile("global_store_dwordx4 %0, %1, %2 sc1\n\ts_nop 1" ::"v"(lane * 16), "v"(d), "s"(gbase + (r * 32 + mem * 8 + blk * 4 + u) * 1024) : "memory");
      }
      STAMP(7 + 3 * blk);
      waitv<0>();
      if (lane == 0) {
        const u32 old = __hip_atomic_fetch_add(((LAS u32*)shm) + (CTL + 8) / 4 + blk, 1u, __ATOMIC_RELAXED, __HIP_MEMORY_SCOPE_WORKGROUP);
        if (old == 3) __hip_atomic_store(flags + mem * 2 + blk, tag, RLX_AGENT);
      }
      STAMP(8 + 3 * blk);
    }
  } else {
    if (w == 0 && lane == 0) {
      const u64 G = __hip_atomic_load(flags + 16, RLX_AGENT);
      u64 tg = (G ^ (G >> 29)) * 0x9E3779B97F4A7C15ull + 0x632BE59BD9B4E019ull;
      if (tg == G) ++tg;
      *(LAS u64*)(shm + CTL) = tg;
      *(LAS u64*)(shm + CTL + 8) = 0;
    }
    const int qq = l15 >> 2, p = lane & 3;
    const int xb = XOFF + l15 * 128 + ((q ^ (l15 >> 1)) << 4);
    int wb[2];
#pragma unroll
    for (int t = 0; t < 2; ++t) wb[t] = 256 * (8 * q + 4 * t + qq) + (((2 * u + (p >> 1)) ^ ((qq << 2) | ((2 * q + t) & 3))) << 4) + 8 * (p & 1);
    f32x4 acc[9][4];
#pragma unroll
    for (int m = 0; m < 9; ++m)
#pragma unroll
      for (int n = 0; n < 4; ++n) acc[m][n] = f32x4{0, 0, 0, 0};
    bf8 xf[9], wf[2][4];
    auto ldw = [&](int set, int n, int slot) {
      char* base = shm + slot + (n & 1) * 8192;
      wf[set][n] = ldtr(base + (wb[0] ^ ((n >> 1) * 128)), base + (wb[1] ^ ((n >> 1) * 128)));
    };
    waitl0();
    bar();
    PAD(1);
    STAMP(1);
#pragma unroll
    for (int n = 0; n < 4; ++n) ldw(0, n, 0);
#pragma unroll
    for (int m = 0; m < 4; ++m) xf[m] = ldb128(shm + xb + m * 2048);
    int xs = 0, wsn = WS;
#pragma unroll 1
    for (int P = 0; P < 8; ++P) {
      const int xsn = xs == 2 * XS ? 0 : xs + XS;
#pragma unroll
      for (int ks = 0; ks < 2; ++ks) {
        const char* xc = shm + ((xb + xs) ^ (ks * 64));
        const char* xn = ks == 0 ? shm + ((xb + xs) ^ 64) : shm + xb + xsn;
#pragma unroll
        for (int m = 0; m < 9; ++m) {
          sbar();
          if (m == 5) bar();
          if (m < 5) xf[m + 4] = ldb128(xc + (m + 4) * 2048);
          if (m >= 5) ldw(ks ^ 1, m - 5, wsn);
          const bf8 xv = xf[m];
          if (m >= 5) xf[m - 5] = ldb128(xn + (m - 5) * 2048);
          sbar();
#pragma unroll
          for (int n = 0; n < 4; ++n) acc[m][n] = __builtin_amdgcn_mfma_f32_16x16x32_bf16(xv, wf[ks][n], acc[m][n], 0, 0, 0);
        }
        wsn = wsn == 5 * WS ? 0 : wsn + WS;
      }
      xs = xsn;
    }
    sbar();
    PAD(5);
    STAMP(5);
#pragma unroll
    for (int blk = 0; blk < 2; ++blk) {
      char* him = shm + XOFF + (blk ? 0 : 2 * XS) + u * (16 * HSTR) + l15 * HSTR + ((((q >> 1) ^ ((l15 >> 2) & 1)) << 4) | (((q & 1) ^ (l15 >> 3)) << 3));
#pragma unroll
      for (int m = 0; m < 9; ++m) {
        const f32x4 a = acc[m][2 * blk], g = acc[m][2 * blk + 1];
        *(u32x2*)(him + m * 32) = u32x2{pk2(a[0] * sigm(g[0]), a[1] * sigm(g[1])), pk2(a[2] * sigm(g[2]), a[3] * sigm(g[3]))};
      }
      STAMP(6 + 3 * blk);
      sbar();
      waitl0();
      bar();
      sbar();
      STAMP(7 + 3 * blk);
      if (blk == 0) { vunit(5, 3 * WS); vunit(6, XOFF + XS); }
      else vunit(7, XOFF + 2 * XS);
    }
  }
  STAMP(12);
  const int role = w >> 2;
  const u64 tag = *(LAS u64*)(shm + CTL);
  {
    GAS u64* fp = flags + (lane & 3) * 2 + role;
    for (int it = 0; it < (1 << 20); ++it) {
      const u64 f = __hip_atomic_load(fp, RLX_AGENT);
      if (__all(f == tag)) break;
      __builtin_amdgcn_s_sleep(1);
    }
  }
  PAD(13);
  STAMP(13);
  if (w == 0 && mem == 0 && lane == 0) __hip_atomic_store(flags + 16, tag, RLX_AGENT);
  u32x4 gf[16];
  {
    const u32 goff = lane * 16;
#pragma unroll
    for (int n = 0; n < 4; ++n) {
      const char* gc = gbase + (u * 32 + ((mem + n) & 3) * 8 + role * 4) * 1024;
      asm volatile("global_load_dwordx4 %0, %4, %5 sc1\n\tglobal_load_dwordx4 %1, %4, %5 offset:1024 sc1\n\t"
                   "global_load_dwordx4 %2, %4, %5 offset:2048 sc1\n\tglobal_load_dwordx4 %3, %4, %5 offset:3072 sc1"
                   : "=&v"(gf[4 * n]), "=&v"(gf[4 * n + 1]), "=&v"(gf[4 * n + 2]), "=&v"(gf[4 * n + 3])
                   : "v"(goff), "s"(gc) : "memory");
    }
  }
  if (role) bar();
  const int h = lane >> 5, rho = lane & 31;
  int tb[2];
  {
    const int blk = q & 1, qq = l15 >> 2, p = lane & 3;
#pragma unroll
    for (int t = 0; t < 2; ++t) tb[t] = 256 * (8 * h + 4 * t + qq) + (((2 * blk + (p >> 1)) ^ ((qq << 2) | ((2 * h + t) & 3))) << 4) + 8 * (p & 1);
  }
  int vs[4];
  vs[0] = role ? 2 * WS : 4 * WS; vs[1] = role ? 3 * WS : 5 * WS; vs[2] = role ? XOFF + XS : 0; vs[3] = role ? XOFF + 2 * XS : WS;
  f32x16 acc2[4];
#pragma unroll
  for (int c = 0; c < 4; ++c)
#pragma unroll
    for (int r = 0; r < 16; ++r) acc2[c][r] = 0.f;
  bf8 af[2][4];
  auto lda = [&](int set, int step) {
    char* base = shm + vs[step >> 2] + (step & 3) * 4096;
#pragma unroll
    for (int n = 0; n < 4; ++n) af[set][n] = ldtr(base + (tb[0] ^ (n * 64)), base + (tb[1] ^ (n * 64)));
  };
  lda(0, 0);
#pragma unroll
  for (int step = 0; step < 16; ++step) {
    sbar();
    if (step < 15) lda((step + 1) & 1, step + 1);
    if (step == 0) { waitv<12>(); STAMP(14); }
    if (step == 4) waitv<8>();
    if (step == 8) waitv<4>();
    if (step == 12) waitv<0>();
    if ((step & 3) == 0) asm volatile("" : "+v"(gf[step]), "+v"(gf[step + 1]), "+v"(gf[step + 2]), "+v"(gf[step + 3]));
    sbar();
    const bf8 bfr = __builtin_bit_cast(bf8, gf[step]);
#pragma unroll
    for (int n = 0; n < 4; ++n) acc2[n] = __builtin_amdgcn_mfma_f32_32x32x16_bf16(af[step & 1][n], bfr, acc2[n], 0, 0, 0);
  }
  sbar();
  STAMP(15);
  char* pb = shm + (u < 2 ? (4 + u) * WS : (u - 2) * WS);
  if (!role) {
    bar();
#pragma unroll
    for (int n = 0; n < 4; ++n)
#pragma unroll
      for (int a = 0; a < 4; ++a)
        *(f32x4*)(pb + (n * 4 + a) * 1024 + lane * 16) = f32x4{acc2[n][4 * a], acc2[n][4 * a + 1], acc2[n][4 * a + 2], acc2[n][4 * a + 3]};
    waitl0();
    bar();
    STAMP(16);
    STAMP_OUT;
    return;
  }
  bar();
  PAD(16);
  STAMP(16);
  u32x2 o[16];
#pragma unroll
  for (int n = 0; n < 4; ++n)
#pragma unroll
    for (int a = 0; a < 4; ++a) {
      const f32x4 v = *(const f32x4*)(pb + (n * 4 + a) * 1024 + lane * 16);
      o[n * 4 + a] = u32x2{pk2(acc2[n][4 * a] + v[0], acc2[n][4 * a + 1] + v[1]), pk2(acc2[n][4 * a + 2] + v[2], acc2[n][4 * a + 3] + v[3])};
    }
  sbar();
#pragma unroll
  for (int i = 0; i < 16; ++i) *(u32x2*)(pb + rho * 256 + ((i ^ (rho & 15)) << 4) + 8 * (h ^ (rho >> 4))) = o[i];
  char* orow = out + ((size_t)batch * 4000 + t0) * 1024 + mem * 256 + l15 * 16;
#pragma unroll
  for (int i = 0; i < 8; ++i) {
    const int r = 4 * i + q;
    u32x4 v = *(const u32x4*)(pb + r * 256 + ((l15 ^ (r & 15)) << 4));
    if (i >= 4) v = u32x4{v[2], v[3], v[0], v[1]};
    const int j = 8 * (r & 15) + 4 * (r >> 4) + u;
    if (j < 125) asm volatile("global_store_dwordx4 %0, %1, off sc1\n\ts_nop 1" ::"v"(orow + j * 1024), "v"(v) : "memory");
  }
  STAMP(17);
  if (PADAT == 18) waitv<0>();
  PAD(18);
  STAMP(18);
  STAMP_OUT;
}

extern "C" void kernel_launch(void* const* d_in, const int* in_sizes, int n_in, void* d_out, int out_size, void* d_ws,
                              size_t ws_size, hipStream_t stream) {
  (void)hipFuncSetAttribute((const void*)k_conv_module, hipFuncAttributeMaxDynamicSharedMemorySize, LDSZ);
  k_conv_module<<<256, 512, LDSZ, stream>>>((const char*)d_in[0], (const char*)d_in[1], (const char*)d_in[2], (const char*)d_in[3],
                                            (char*)d_ws, (char*)d_out);
}
```

```cpp
#include <hip/hip_runtime.h>
#include <hip/hip_bf16.h>

typedef unsigned u32;
typedef unsigned long long u64;
typedef __attribute__((ext_vector_type(2))) unsigned u32x2;
typedef __attribute__((ext_vector_type(4))) unsigned u32x4;
typedef __attribute__((ext_vector_type(2))) unsigned long long u64x2;
typedef __attribute__((ext_vector_type(4))) unsigned long long u64x4;
typedef __attribute__((ext_vector_type(2))) __bf16 bf2;
typedef __attribute__((ext_vector_type(4))) __bf16 bf4;
typedef __attribute__((ext_vector_type(8))) __bf16 bf8;
typedef __attribute__((ext_vector_type(2))) float f32x2;
typedef __attribute__((ext_vector_type(4))) float f32x4;
#define LAS __attribute__((address_space(3)))
#define GAS __attribute__((address_space(1)))
#define DEVI __device__ __forceinline__
#define RLX_AGENT __ATOMIC_RELAXED, __HIP_MEMORY_SCOPE_AGENT

#define ROT 0
#define G0AT 12
#define G1AT 40

template <int N> DEVI void waitv() { asm volatile("s_waitcnt vmcnt(%0)" ::"n"(N) : "memory"); }
DEVI void sbar() { __builtin_amdgcn_sched_barrier(0); }
DEVI void waitl0() { sbar(); __builtin_amdgcn_s_waitcnt(0xC07F); sbar(); }
#define PINV(x) asm volatile("" : "+v"(x))
#define PINS(x) asm volatile("" : "+s"(x))
DEVI void bar() { asm volatile("s_barrier" ::: "memory"); }

DEVI u32 pk2(float a, float b) {
  f32x2 v = {a, b};
  bf2 r = __builtin_convertvector(v, bf2);
  return __builtin_bit_cast(u32, r);
}
DEVI f32x2 gate2(f32x2 a, f32x2 b) {
  const f32x2 t = b * -1.4426950408889634f;
  f32x2 e = {__builtin_amdgcn_exp2f(t[0]), __builtin_amdgcn_exp2f(t[1])};
  e = e + 1.0f;
  const f32x2 r = {__builtin_amdgcn_rcpf(e[0]), __builtin_amdgcn_rcpf(e[1])};
  return a * r;
}
#define LP(T, a) ((LAS T*)(size_t)(a))
DEVI bf8 ld128(u32 a) { return *LP(const bf8, a); }
DEVI bf4 ldtr(u32 a) { return __builtin_amdgcn_ds_read_tr16_b64_v4bf16(LP(bf4, a)); }
DEVI bf8 cat(bf4 a, bf4 b) { return __builtin_shufflevector(a, b, 0, 1, 2, 3, 4, 5, 6, 7); }
DEVI f32x4 mfma(bf8 a, bf8 b, f32x4 c) { return __builtin_amdgcn_mfma_f32_16x16x32_bf16(a, b, c, 0, 0, 0); }
template <int D, int S> DEVI void glds(__amdgpu_buffer_rsrc_t r, u32 dst, u32 voff, u32 soff) {
  u32 t;
  asm volatile("s_add_u32 m0, %1, %5\n\ts_add_u32 %0, %4, %6\n\tbuffer_load_dwordx4 %2, %3, %0 offen lds"
               : "=&s"(t) : "s"(dst), "v"(voff), "s"(r), "s"(soff), "n"(D), "n"(S) : "memory", "scc");
}
DEVI void gst(u32 voff, u32x4 d, char* base) {
  asm volatile("global_store_dwordx4 %0, %1, %2 sc1\n\ts_nop 1" ::"v"(voff), "v"(d), "s"(base) : "memory");
}
template <int O> DEVI void gsto(u32 voff, u32x4 d, __amdgpu_buffer_rsrc_t r) {
  asm volatile("buffer_store_dwordx4 %1, %0, %2, 0 offen offset:%3 sc1" ::"v"(voff), "v"(d), "s"(r), "n"(O) : "memory");
}
DEVI void mfma_acc(f32x4& c, bf8 a, u32x4 b) { asm volatile("v_mfma_f32_16x16x32_bf16 %0, %1, %2, %0" : "+v"(c) : "v"(a), "v"(b)); }
DEVI void mfma_new(f32x4& c, bf8 a, u32x4 b) { asm volatile("v_mfma_f32_16x16x32_bf16 %0, %1, %2, 0" : "=&v"(c) : "v"(a), "v"(b)); }
DEVI void gld4(u32x4* g, u32 voff, const char* base) {
  asm volatile("global_load_dwordx4 %0, %4, %5 sc1\n\tglobal_load_dwordx4 %1, %4, %5 offset:256 sc1\n\t"
               "global_load_dwordx4 %2, %4, %5 offset:2048 sc1\n\tglobal_load_dwordx4 %3, %4, %5 offset:2304 sc1"
               : "=&v"(g[0]), "=&v"(g[1]), "=&v"(g[2]), "=&v"(g[3])
               : "v"(voff), "s"(base) : "memory");
}
template <int V> struct IC { static constexpr int value = V; };
template <int I, int N, class Fn> DEVI void sfor(Fn&& f) {
  if constexpr (I < N) { f(IC<I>{}); sfor<I + 1, N>(f); }
}
DEVI u32 max3(u32 a, u32 b, u32 c) { return max(max(a, b), c); }

constexpr int SL = 51200;
constexpr int XO = 32768;
constexpr int DWOFF = 3 * SL;
constexpr int DUMMY = DWOFF + 4096;
constexpr int CTL = DWOFF + 5120;
constexpr int LDSZ = 163840;
constexpr int HSTR = 288;
constexpr int H0 = 2 * SL + XO, H1 = 0;
constexpr int BH = 69632, BL = 18432;
constexpr int VOFF[8] = {BH + 32768, BH + 49152, BL, BL + 16384, BL + 34816, BH, BH + 16384, BH + 32768};
constexpr int GSTR = 131072 + 256;

#define STAMP(i)
#define STAMP_INIT
#define STAMP_OUT

constexpr int fV(int F) { return F < 80 ? F >> 4 : 5 + ((F - 80) % 12) / 4; }
constexpr int fK(int F) { return F < 80 ? (F >> 3) & 1 : (((F - 80) % 12) >> 1) & 1; }
constexpr int fJ(int F) { return F < 80 ? F & 7 : 2 * ((F - 80) / 12) + (F & 1); }

__global__ __launch_bounds__(512, 2) void k_conv_module(const char* x, const char* w1,
                                                        const char* dw, const char* w2,
                                                        char* ws, char* out) {
  u64x4 ka;
  u64 kb;
  asm volatile("s_load_dwordx8 %0, %2, 0x0\n\ts_load_dwordx2 %1, %2, 0x20" : "=&s"(ka), "=&s"(kb) : "s"(__builtin_amdgcn_kernarg_segment_ptr()));
  sbar();
  const int tid = threadIdx.x, lane = tid & 63;
  const int w = __builtin_amdgcn_readfirstlane(tid >> 6);
  STAMP_INIT;
  STAMP(0);

  const int u = w & 3;
  const int b = blockIdx.x, xcd = b & 7, idx = b >> 3, gidx = idx >> 2, mem = idx & 3;
  const int grp = xcd * 8 + gidx, batch = grp >> 5, tile = grp & 31;
  const int t0 = tile * 125;
  const int k0 = ROT ? ((2 * mem + gidx) & 7) : 0;
  const int l15 = lane & 15, q = lane >> 4;
  if (w >= 4) {
    __builtin_amdgcn_s_setprio(3);
    u32 lb = u * 1024;
    u32 xo[5];
    xo[0] = (u32)((t0 - 8 + 8 * u + (lane >> 3)) * 1024 + (((lane & 7) ^ ((4 * u + q) & 7)) << 4));
#pragma unroll
    for (int i = 1; i < 5; ++i) xo[i] = xo[0] + i * 32768;
    if (u >= 2) xo[4] = 0x80000000u;
    const u32 swz = (u32)(mem * 256 + ((l15 ^ ((q << 2) | (u & 2))) << 4));
    u32 woff0 = (u32)((4 * u + q) * 2048) + swz;
    u32 voff0 = (u32)((4 * u + q) * 1024) + swz;
    u32 doff = (u32)((lane >> 2) * 1024 + mem * 256 + ((lane >> 1) & 1) * 128 + u * 32 + (lane & 1) * 16);
    u32 x5 = u < 2 ? lb + XO + 16384 : DUMMY, x5s = u < 2 ? SL : 0, xb = batch * 4096000;
    PINS(lb); PINS(x5); PINS(x5s); PINS(xb); PINV(woff0); PINV(voff0); PINV(doff);
#pragma unroll
    for (int i = 0; i < 5; ++i) PINV(xo[i]);
    sbar();
    asm volatile("s_waitcnt lgkmcnt(0)\n\ts_mov_b64 %0, %5\n\ts_mov_b64 %1, %6\n\ts_mov_b64 %2, %7\n\ts_mov_b64 %3, %8\n\ts_mov_b64 %4, %9"
                 : "=&s"(x), "=&s"(w1), "=&s"(dw), "=&s"(w2), "=&s"(ws) : "s"(ka[0]), "s"(ka[1]), "s"(ka[2]), "s"(ka[3]), "s"(kb));
    sbar();
    char* gbase = ws + (size_t)grp * GSTR;
    char* flags = gbase + 131072;
    const __amdgpu_buffer_rsrc_t rx = __builtin_amdgcn_make_buffer_rsrc((void*)(x + xb), 0, 4096000, 0x00020000);
    const __amdgpu_buffer_rsrc_t rw = __builtin_amdgcn_make_buffer_rsrc((void*)w1, 0, 1048576, 0x00020000);
    const __amdgpu_buffer_rsrc_t rd = __builtin_amdgcn_make_buffer_rsrc((void*)dw, 0, 15360, 0x00020000);
    const __amdgpu_buffer_rsrc_t rv = __builtin_amdgcn_make_buffer_rsrc((void*)w2, 0, 524288, 0x00020000);
    auto xunit = [&](auto Pc) {
      constexpr int P = Pc.value, d = XO + (P % 3) * SL, sc = ROT ? 0 : P * 128;
      const u32 so = ROT ? ((k0 + P) & 7) * 128 : 0;
      glds<d, sc>(rx, lb, xo[0], so);
      glds<d + 4096, sc>(rx, lb, xo[1], so);
      glds<d + 8192, sc>(rx, lb, xo[2], so);
      glds<d + 12288, sc>(rx, lb, xo[3], so);
      glds<0, sc>(rx, x5 + (P % 3) * x5s, xo[4], so);
    };
    auto wunit = [&](auto Pc, auto kc) {
      constexpr int P = Pc.value, ks = kc.value, d = (P % 3) * SL + ks * 16384, sc = (ROT ? 0 : P * 131072) + ks * 65536;
      const u32 so = ROT ? ((k0 + P) & 7) * 131072 : 0;
      glds<d, sc>(rw, lb, woff0, so);
      glds<d + 8192, sc + 1024>(rw, lb, woff0, so);
      glds<d + 4096, sc + 32768>(rw, lb, woff0, so);
      glds<d + 12288, sc + 32768 + 1024>(rw, lb, woff0, so);
    };
    auto punit = [&](auto Pc) { xunit(Pc); wunit(Pc, IC<0>{}); wunit(Pc, IC<1>{}); };
    auto vunit = [&](auto nc) {
      constexpr int n = nc.value;
      const u32 c = n < 2 ? 2 * mem : 2 * ((mem + 1 + (n - 2) % 3) & 3);
      constexpr int o = (n < 2 ? n : n >= 5) * 65536;
      glds<VOFF[n], o>(rv, lb, voff0, c * 65536);
      glds<VOFF[n] + 4096, o + 16384>(rv, lb, voff0, c * 65536);
      glds<VOFF[n] + 8192, o + 32768>(rv, lb, voff0, c * 65536);
      glds<VOFF[n] + 12288, o + 49152>(rv, lb, voff0, c * 65536);
    };
    char* gdst = gbase + (mem * 8 + u) * 1024;
    auto poison = [&](int blk) {
#pragma unroll
      for (int r = 0; r < 4; ++r) gst(lane * 16, u32x4{~0u, ~0u, ~0u, ~0u}, gdst + r * 32768 + blk * 4096);
    };
    u32x4 T[2][2][8];
    auto tbuild = [&](int blk, int hi) {
      u32x4 v[8];
#pragma unroll
      for (int j = 0; j < 8; ++j) {
        const int jj = j ^ (hi << 2);
        u32 i = (u32)(8 * q + jj - (l15 & 7) - 1);
        if ((l15 >> 3) != hi) i = 15;
        v[j] = *LP(const u32x4, DWOFF + lb + (min(i, 15u) << 6) + blk * 32 + hi * 16);
      }
#pragma unroll
      for (int c = 0; c < 8; ++c)
#pragma unroll
        for (int m = 0; m < 4; ++m)
          T[blk][hi][c][m] = __builtin_amdgcn_perm(v[2 * m + 1][c >> 1], v[2 * m][c >> 1], (c & 1) ? 0x07060302u : 0x05040100u);
#pragma unroll
      for (int c = 0; c < 8; ++c) asm volatile("" : "+v"(T[blk][hi][c]));
    };
    punit(IC<0>{});
    glds<DWOFF, 0>(rd, lb, doff, 0);
    punit(IC<1>{});
    __builtin_amdgcn_s_setprio(0);
    waitv<18>(); bar();
    STAMP(1);
    xunit(IC<2>{}); waitv<19>(); bar();
    wunit(IC<2>{}, IC<0>{}); wunit(IC<2>{}, IC<1>{}); waitv<17>(); bar();
    xunit(IC<3>{}); waitv<18>(); bar();
    wunit(IC<3>{}, IC<0>{}); wunit(IC<3>{}, IC<1>{}); waitv<13>(); bar();
    STAMP(2);
    u64 G;
    punit(IC<4>{}); poison(0);
    asm volatile("global_load_dwordx2 %0, %1, %2 offset:128 sc1" : "=&v"(G) : "v"(0), "s"(flags) : "memory");
    tbuild(0, 0); waitv<18>(); bar();
    punit(IC<5>{}); poison(1); tbuild(0, 1); waitv<22>(); bar();
    STAMP(3);
    punit(IC<6>{}); tbuild(1, 0); waitv<17>(); bar();
    PINV(G);
    punit(IC<7>{});
    u64 tag = (G ^ (G >> 29)) * 0x9E3779B97F4A7C15ull + 0x632BE59BD9B4E019ull;
    if (tag == G) ++tag;
    PINV(tag);
    tbuild(1, 1); waitv<13>(); bar();
    STAMP(4);
    if (u == 0 && lane == 0) asm volatile("global_store_dwordx2 %0, %1, %2 sc1" ::"v"(mem * 8), "v"(tag), "s"(flags) : "memory");
    vunit(IC<0>{}); vunit(IC<1>{}); waitv<8>(); bar();
    vunit(IC<2>{}); vunit(IC<3>{}); bar();
    STAMP(5);
    vunit(IC<4>{}); vunit(IC<5>{}); vunit(IC<6>{});
    __builtin_amdgcn_s_setprio(1);
    u32 hr[2][2], hw[2];
    char* gd[2][4];
#pragma unroll
    for (int blk = 0; blk < 2; ++blk) {
      const u32 him = (blk ? H1 : H0) + u * (16 * HSTR);
      const int hoff = ((q == 3 ? 0 : q) + l15) * 16;
      hr[blk][0] = him + hoff; hr[blk][1] = him + (hoff ^ 16); hw[blk] = him + lane * 16;
      PINV(hr[blk][0]); PINV(hr[blk][1]); PINV(hw[blk]);
#pragma unroll
      for (int r = 0; r < 4; ++r) { gd[blk][r] = gdst + r * 32768 + blk * 4096; PINS(gd[blk][r]); }
    }
    u32 l16 = lane * 16;
    PINV(l16);
    waitv<20>();
#pragma unroll
    for (int blk = 0; blk < 2; ++blk) {
      sbar();
      bar();
      STAMP(6 + 3 * blk);
      bf8 wv[16];
#pragma unroll
      for (int c = 0; c < 8; ++c) {
        wv[c] = ld128(hr[blk][c >> 2] + c * HSTR);
        wv[c + 8] = ld128(hr[blk][c >> 2] + (c + 8) * HSTR);
      }
      sbar();
      f32x4 sv[8];
#pragma unroll
      for (int c = 0; c < 8; ++c) {
        f32x4 d = mfma(__builtin_bit_cast(bf8, T[blk][0][c]), wv[c], f32x4{0, 0, 0, 0});
        sv[c] = mfma(__builtin_bit_cast(bf8, T[blk][1][c]), wv[c + 8], d);
      }
#pragma unroll
      for (int c = 0; c < 8; ++c) {
        const f32x2 lo = gate2(sv[c].xy, sv[c].xy), hi = gate2(sv[c].zw, sv[c].zw);
        sv[c] = f32x4{lo[0], lo[1], hi[0], hi[1]};
      }
#pragma unroll
      for (int r = 0; r < 4; ++r) {
        const u32x4 d = u32x4{min(pk2(sv[0][r], sv[1][r]), ~1u), min(pk2(sv[2][r], sv[3][r]), ~1u),
                              min(pk2(sv[4][r], sv[5][r]), ~1u), min(pk2(sv[6][r], sv[7][r]), ~1u)};
        gst(l16, d, gd[blk][r]);
        *LP(u32x4, hw[blk] + r * 1024) = d;
      }
      STAMP(7 + 3 * blk);
      if (blk == 0) waitv<16>(); else waitv<8>();
      waitl0();
    }
    bar();
    STAMP(12);
    vunit(IC<7>{});
    waitv<0>();
    STAMP(13);
    bar();
    STAMP_OUT;
    return;
  }
  waitl0();
  const int qq = l15 >> 2, p = lane & 3;
  u32 trb = 256 * (8 * q + qq) + ((((p >> 1) ^ ((qq << 2) | ((q & 1) << 1)))) << 4) + 8 * (p & 1);
  u64 fl;
  char* gbase = ws + (size_t)grp * GSTR;
  char* flags = gbase + 131072;
  u32 va[8];
#pragma unroll
  for (int j = 0; j < 8; ++j) { va[j] = (trb ^ (j << 5)) + BH; PINV(va[j]); }
  u32 goff = (q >> 1) * 1024 + (q & 1) * 512 + l15 * 16;
  u32 gla = (q >> 1) * (16 * HSTR) + u * 1024 + (q & 1) * 512 + l15 * 16, gla0 = gla + H0;
  PINV(goff); PINV(gla); PINV(gla0);
  const char* gs[8];
#pragma unroll
  for (int n = 2; n < 8; ++n) { gs[n] = gbase + (u * 32 + ((mem + 1 + (n - 2) % 3) & 3) * 8 + (n >= 5) * 4) * 1024; PINS(gs[n]); }
  u32 ooff[2];
  ooff[0] = (l15 * 8 + u) * 1024 + (q & 1) * 32 + (q >> 1) * 16;
  ooff[1] = ooff[0] + 4096;
  PINV(ooff[0]); PINV(ooff[1]);
  __amdgpu_buffer_rsrc_t ro = __builtin_amdgcn_make_buffer_rsrc(out + ((size_t)batch * 4000 + t0) * 1024 + mem * 256, 0, 124 * 1024 + 256, 0x00020000);
  PINS(ro); PINS(flags);
  {
    u32 xa0 = XO + l15 * 128 + ((q ^ (l15 >> 1)) << 4), xa1 = xa0 ^ 64;
    u32 wa0 = trb ^ (u << 5), wa1 = wa0 ^ 128;
    f32x4 acc[9][4];
#pragma unroll
    for (int m = 0; m < 9; ++m)
#pragma unroll
      for (int n = 0; n < 4; ++n) { acc[m][n] = f32x4{0, 0, 0, 0}; PINV(acc[m][n]); }
    u32 hg = u * (16 * HSTR) + l15 * HSTR + ((((q >> 1) ^ ((l15 >> 2) & 1)) << 4) | (((q & 1) ^ (l15 >> 3)) << 3));
    u32 hg0 = hg + H0, fo = (lane & 3) * 8;
    PINV(hg); PINV(hg0); PINV(fo); PINV(xa0); PINV(xa1); PINV(wa0); PINV(wa1);
    bf8 xf[9];
    bf4 wf[2][4][2];
    __builtin_amdgcn_s_setprio(2);
    waitl0();
    bar();
    STAMP(1);
#pragma unroll
    for (int n = 0; n < 4; ++n) {
      wf[0][n][0] = ldtr((n >> 1 ? wa1 : wa0) + (n & 1) * 8192);
      wf[0][n][1] = ldtr((n >> 1 ? wa1 : wa0) + (n & 1) * 8192 + 1024);
      if (n == 0) xf[0] = ld128(xa0);
    }
#pragma unroll
    for (int m = 1; m < 8; ++m) xf[m] = ld128(xa0 + m * 2048);
    int sl = 0, dlt = 0;
#pragma unroll 1
    for (int P = 0; P < 8; ++P) {
#pragma unroll
      for (int ks = 0; ks < 2; ++ks) {
#pragma unroll
        for (int m = 0; m < 9; ++m) {
#pragma unroll
          for (int n = 0; n < 4; ++n) {
            sbar();
            const int s = m * 4 + n;
            if (s == 16) {
              if (ks == 0) { if (P < 2) bar(); }
              else { waitl0(); bar(); }
            }
            const bf8 xv = xf[m];
            const u32 xn = ks ? xa0 : xa1;
            const int wo = ks ? 0 : 16384;
            auto rw_ = [&](int n2, int t) { wf[ks ^ 1][n2][t] = ldtr((n2 >> 1 ? wa1 : wa0) + wo + (n2 & 1) * 8192 + t * 1024); };
            auto rx_ = [&](int m2) { xf[m2] = ld128(xn + m2 * 2048); };
            if (s == 1) xf[8] = ld128((ks ? xa1 : xa0) + 8 * 2048);
            if (ks == 1) {
              if (s == 2) { dlt = sl == 2 ? -2 * SL : SL; PINS(dlt); }
              if (s == 3) { sl = sl == 2 ? 0 : sl + 1; PINS(sl); }
              if (s == 5) { xa0 += dlt; asm volatile("" : "+v"(xa0)); }
              if (s == 6) { xa1 += dlt; asm volatile("" : "+v"(xa1)); }
              if (s == 9) { wa0 += dlt; asm volatile("" : "+v"(wa0)); }
              if (s == 10) { wa1 += dlt; asm volatile("" : "+v"(wa1)); }
            }
            if (s == 17) rw_(0, 0);
            if (s == 18) rw_(0, 1);
            if (s == 19) rx_(0);
            if (s == 20) rw_(1, 0);
            if (s == 21) rw_(1, 1);
            if (s == 22) rx_(1);
            if (s == 23) rw_(2, 0);
            if (s == 24) rw_(2, 1);
            if (s == 25) rx_(2);
            if (s == 26) rw_(3, 0);
            if (s == 27) rw_(3, 1);
            if (s == 28) rx_(3);
            if (s == 29) rx_(4);
            if (s == 30) rx_(5);
            if (s == 31) rx_(6);
            if (s == 33) rx_(7);
            sbar();
            acc[m][n] = mfma(xv, cat(wf[ks][n][0], wf[ks][n][1]), acc[m][n]);
          }
        }
      }
    }
    sbar();
    __builtin_amdgcn_s_setprio(0);
    STAMP(5);
    asm volatile("global_load_dwordx2 %0, %1, %2 sc1" : "=&v"(fl) : "v"(fo), "s"(flags) : "memory");
    sbar();
#pragma unroll
    for (int blk = 0; blk < 2; ++blk) {
      const u32 him = blk ? hg + H1 : hg0;
#pragma unroll
      for (int m = 0; m < 9; ++m) {
        const f32x4 a = acc[m][2 * blk], g = acc[m][2 * blk + 1];
        const f32x2 lo = gate2(a.xy, g.xy), hi = gate2(a.zw, g.zw);
        *LP(u32x2, him + m * 32) = u32x2{pk2(lo[0], lo[1]), pk2(hi[0], hi[1])};
      }
      STAMP(6 + 3 * blk);
      sbar();
      if (blk == 0) {
        waitl0();
        bar();
        sbar();
        STAMP(7);
      }
    }
  }
  waitv<0>();
  PINV(fl);
  auto alleq = [&]() {
    const u64 f0 = ((u64)__builtin_amdgcn_readfirstlane((u32)(fl >> 32)) << 32) | (u32)__builtin_amdgcn_readfirstlane((u32)fl);
    return __all(fl == f0);
  };
  if (__builtin_expect(!alleq(), 0)) {
#pragma unroll 1
    for (int it = 0; it < (1 << 20); ++it) {
      __builtin_amdgcn_s_sleep(1);
      asm volatile("global_load_dwordx2 %0, %1, %2 sc1\n\ts_waitcnt vmcnt(0)" : "=&v"(fl) : "v"((lane & 3) * 8), "s"(flags) : "memory");
      if (alleq()) break;
    }
  }
  if (w == 0 && mem == 0 && lane == 0) asm volatile("global_store_dwordx2 %0, %1, %2 offset:128 sc1" ::"v"(0), "v"(fl), "s"(flags) : "memory");
  f32x4 acc2[2][8];
  bf4 af[8][2];
  u32x4 g[8][4];
  auto gsrc = [&](int n) { return gs[n]; };
  auto rdaf = [&](int F, int t) {
    const int v = fV(F);
    af[F & 7][t] = ldtr(va[fJ(F)] + (VOFF[v] - (v >= 2 && v <= 4 ? BL : BH)) + fK(F) * 8192 + t * 1024);
  };
  u32 mx = 0;
  auto settle = [&](int n) {
    if (__builtin_expect(__any(mx == ~0u), 0)) {
#pragma unroll 1
      for (int it = 0; it < (1 << 18); ++it) {
        gld4(g[n], goff, gsrc(n));
        waitv<0>();
        asm volatile("" : "+v"(g[n][0]), "+v"(g[n][1]), "+v"(g[n][2]), "+v"(g[n][3]));
        u32 m2 = 0;
#pragma unroll
        for (int i = 0; i < 8; ++i) m2 = max3(m2, g[n][i >> 1][(i & 1) * 2], g[n][i >> 1][(i & 1) * 2 + 1]);
        if (!__any(m2 == ~0u)) break;
      }
    }
  };
#pragma unroll
  for (int F = 0; F < 7; ++F) { rdaf(F, 0); rdaf(F, 1); }
  u32x4 od[2];
  sbar();
  waitl0();
  bar();
  STAMP(10);
  sfor<0, 128>([&](auto Fc) {
    constexpr int F = Fc.value;
    constexpr int v = fV(F), j = fJ(F), kk = fK(F);
    sbar();
    if (F == 16) { STAMP(11); bar(); STAMP(12); }
    if (F == 80) { STAMP(16); bar(); STAMP(17); }
    if (F == 0 || F == 16) {
#pragma unroll
      for (int i = 0; i < 4; ++i) g[v][i] = __builtin_bit_cast(u32x4, ld128((v ? gla + H1 : gla0) + (i >> 1) * (32 * HSTR) + (i & 1) * 256));
    }
    if (F == G0AT) { gld4(g[2], goff, gsrc(2)); gld4(g[3], goff, gsrc(3)); gld4(g[4], goff, gsrc(4)); }
    if (F == G1AT) { gld4(g[5], goff, gsrc(5)); gld4(g[6], goff, gsrc(6)); gld4(g[7], goff, gsrc(7)); }
#pragma unroll
    for (int n = 2; n < 8; ++n) {
      const int c0 = n < 5 ? 16 * n - 4 : 76 + 4 * (n - 5);
      if (F == c0 + 4) { settle(n); if (n == 2) STAMP(14); }
    }
#pragma unroll
    for (int n = 2; n < 8; ++n) {
      const int c0 = n < 5 ? 16 * n - 4 : 76 + 4 * (n - 5);
      if (F == c0) {
        if (n == 2) STAMP(13);
        const int yng = n < 5 ? 4 * (4 - n) + (G1AT <= c0 ? 12 : 0) : 4 * (7 - n);
        if (yng == 20) waitv<20>(); if (yng == 16) waitv<16>(); if (yng == 12) waitv<12>();
        if (yng == 8) waitv<8>(); if (yng == 4) waitv<4>(); if (yng == 0) waitv<0>();
        asm volatile("" : "+v"(g[n][0]), "+v"(g[n][1]), "+v"(g[n][2]), "+v"(g[n][3]));
        mx = 0;
      }
    }
#pragma unroll
    for (int s = 0; s < 2; ++s) {
      sbar();
      if (F + 7 < 128) {
        const int Fn = F + 7;
        rdaf(Fn, s);
        if (s == 1 && Fn >= 24 && Fn < 32) { va[Fn & 7] += BL - BH; asm volatile("" : "+v"(va[Fn & 7])); }
        if (s == 1 && Fn >= 72 && Fn < 80) { va[Fn & 7] += BH - BL; asm volatile("" : "+v"(va[Fn & 7])); }
      }
#pragma unroll
      for (int n = 2; n < 8; ++n) {
        const int c0 = n < 5 ? 16 * n - 4 : 76 + 4 * (n - 5);
        if (F >= c0 && F < c0 + 4) { mx = max3(mx, g[n][F - c0][2 * s], g[n][F - c0][2 * s + 1]); asm volatile("" : "+v"(mx)); }
      }
      if constexpr (F >= 93) {
        constexpr int J = (F - 93) / 12;
        const int e = ((F - 93) % 12) * 2 + s;
        if (e < 16) {
          const int s2 = e >> 3, e2 = e & 7;
          if (e2 < 4) { const f32x4 a = acc2[s2][2 * J + (e2 >> 1)]; od[s2][e2] = pk2(a[2 * (e2 & 1)], a[2 * (e2 & 1) + 1]); asm volatile("" : "+v"(od[s2][e2])); }
          if (e2 == 5 || e2 == 6) { auto r = __builtin_amdgcn_permlane16_swap(od[s2][e2 - 5], od[s2][e2 - 3], false, false); od[s2][e2 - 5] = r[0]; od[s2][e2 - 3] = r[1]; }
          if (e2 == 7) gsto<J * 64>(ooff[s2], od[s2], ro);
        }
      }
      sbar();
      if (F < 8) mfma_new(acc2[s][j], cat(af[F & 7][0], af[F & 7][1]), g[v][kk * 2 + s]);
      else mfma_acc(acc2[s][j], cat(af[F & 7][0], af[F & 7][1]), g[v][kk * 2 + s]);
    }
  });
  sbar();
  asm volatile("s_nop 7\n\ts_nop 3");
  sbar();
  STAMP(18);
#pragma unroll
  for (int s2 = 0; s2 < 2; ++s2) {
#pragma unroll
    for (int e2 = 0; e2 < 4; ++e2) { const f32x4 a = acc2[s2][6 + (e2 >> 1)]; od[s2][e2] = pk2(a[2 * (e2 & 1)], a[2 * (e2 & 1) + 1]); }
#pragma unroll
    for (int e2 = 0; e2 < 2; ++e2) { auto r = __builtin_amdgcn_permlane16_swap(od[s2][e2], od[s2][e2 + 2], false, false); od[s2][e2] = r[0]; od[s2][e2 + 2] = r[1]; }
    gsto<192>(ooff[s2], od[s2], ro);
  }
  STAMP(19);
  STAMP(20);
  STAMP_OUT;
}

extern "C" void kernel_launch(void* const* d_in, const int* in_sizes, int n_in, void* d_out, int out_size, void* d_ws,
                              size_t ws_size, hipStream_t stream) {
  (void)hipFuncSetAttribute((const void*)k_conv_module, hipFuncAttributeMaxDynamicSharedMemorySize, LDSZ);
  k_conv_module<<<256, 512, LDSZ, stream>>>((const char*)d_in[0], (const char*)d_in[1], (const char*)d_in[2], (const char*)d_in[3],
                                            (char*)d_ws, (char*)d_out);
}
```

```cpp
#define PROBE 0
#include <hip/hip_runtime.h>
#include <hip/hip_bf16.h>

typedef unsigned u32;
typedef unsigned long long u64;
typedef __attribute__((ext_vector_type(2))) unsigned u32x2;
typedef __attribute__((ext_vector_type(4))) unsigned u32x4;
typedef __attribute__((ext_vector_type(2))) unsigned long long u64x2;
typedef __attribute__((ext_vector_type(4))) unsigned long long u64x4;
typedef __attribute__((ext_vector_type(2))) __bf16 bf2;
typedef __attribute__((ext_vector_type(4))) __bf16 bf4;
typedef __attribute__((ext_vector_type(8))) __bf16 bf8;
typedef __attribute__((ext_vector_type(2))) float f32x2;
typedef __attribute__((ext_vector_type(4))) float f32x4;
#define LAS __attribute__((address_space(3)))
#define GAS __attribute__((address_space(1)))
#define DEVI __device__ __forceinline__
#define RLX_AGENT __ATOMIC_RELAXED, __HIP_MEMORY_SCOPE_AGENT

#define ROT 0
#define G0AT 12
#define G1AT 40

template <int N> DEVI void waitv() { asm volatile("s_waitcnt vmcnt(%0)" ::"n"(N) : "memory"); }
DEVI void sbar() { __builtin_amdgcn_sched_barrier(0); }
DEVI void waitl0() { sbar(); __builtin_amdgcn_s_waitcnt(0xC07F); sbar(); }
#define PINV(x) asm volatile("" : "+v"(x))
#define PINS(x) asm volatile("" : "+s"(x))
DEVI void bar() { asm volatile("s_barrier" ::: "memory"); }

DEVI u32 pk2(float a, float b) {
  f32x2 v = {a, b};
  bf2 r = __builtin_convertvector(v, bf2);
  return __builtin_bit_cast(u32, r);
}
DEVI f32x2 gate2(f32x2 a, f32x2 b) {
  const f32x2 t = b * -1.4426950408889634f;
  f32x2 e = {__builtin_amdgcn_exp2f(t[0]), __builtin_amdgcn_exp2f(t[1])};
  e = e + 1.0f;
  const f32x2 r = {__builtin_amdgcn_rcpf(e[0]), __builtin_amdgcn_rcpf(e[1])};
  return a * r;
}
#define LP(T, a) ((LAS T*)(size_t)(a))
DEVI bf8 ld128(u32 a) { return *LP(const bf8, a); }
DEVI bf4 ldtr(u32 a) { return __builtin_amdgcn_ds_read_tr16_b64_v4bf16(LP(bf4, a)); }
DEVI bf8 cat(bf4 a, bf4 b) { return __builtin_shufflevector(a, b, 0, 1, 2, 3, 4, 5, 6, 7); }
DEVI f32x4 mfma(bf8 a, bf8 b, f32x4 c) { return __builtin_amdgcn_mfma_f32_16x16x32_bf16(a, b, c, 0, 0, 0); }
template <int D, int S> DEVI void glds(__amdgpu_buffer_rsrc_t r, u32 dst, u32 voff, u32 soff) {
  u32 t;
  asm volatile("s_add_u32 m0, %1, %5\n\ts_add_u32 %0, %4, %6\n\tbuffer_load_dwordx4 %2, %3, %0 offen lds"
               : "=&s"(t) : "s"(dst), "v"(voff), "s"(r), "s"(soff), "n"(D), "n"(S) : "memory", "scc");
}
DEVI void gst(u32 voff, u32x4 d, char* base) {
  asm volatile("global_store_dwordx4 %0, %1, %2 sc1\n\ts_nop 1" ::"v"(voff), "v"(d), "s"(base) : "memory");
}
template <int O> DEVI void gsto(u32 voff, u32x4 d, __amdgpu_buffer_rsrc_t r) {
  asm volatile("buffer_store_dwordx4 %1, %0, %2, 0 offen offset:%3 sc1" ::"v"(voff), "v"(d), "s"(r), "n"(O) : "memory");
}
DEVI void mfma_acc(f32x4& c, bf8 a, u32x4 b) { asm volatile("v_mfma_f32_16x16x32_bf16 %0, %1, %2, %0" : "+v"(c) : "v"(a), "v"(b)); }
DEVI void mfma_new(f32x4& c, bf8 a, u32x4 b) { asm volatile("v_mfma_f32_16x16x32_bf16 %0, %1, %2, 0" : "=&v"(c) : "v"(a), "v"(b)); }
DEVI void gld4(u32x4* g, u32 voff, const char* base) {
  asm volatile("global_load_dwordx4 %0, %4, %5 sc1\n\tglobal_load_dwordx4 %1, %4, %5 offset:256 sc1\n\t"
               "global_load_dwordx4 %2, %4, %5 offset:2048 sc1\n\tglobal_load_dwordx4 %3, %4, %5 offset:2304 sc1"
               : "=&v"(g[0]), "=&v"(g[1]), "=&v"(g[2]), "=&v"(g[3])
               : "v"(voff), "s"(base) : "memory");
}
template <int V> struct IC { static constexpr int value = V; };
template <int I, int N, class Fn> DEVI void sfor(Fn&& f) {
  if constexpr (I < N) { f(IC<I>{}); sfor<I + 1, N>(f); }
}
DEVI u32 max3(u32 a, u32 b, u32 c) { return max(max(a, b), c); }

constexpr int SL = 51200;
constexpr int XO = 32768;
constexpr int DWOFF = 3 * SL;
constexpr int DUMMY = DWOFF + 4096;
constexpr int CTL = DWOFF + 5120;
constexpr int LDSZ = 163840;
constexpr int HSTR = 288;
constexpr int H0 = 2 * SL + XO, H1 = 0;
constexpr int BH = 69632, BL = 18432;
constexpr int VOFF[8] = {BH + 32768, BH + 49152, BL, BL + 16384, BL + 34816, BH, BH + 16384, BH + 32768};
constexpr int GSTR = 131072 + 256;

DEVI u64 rtnow() { u64 t_; asm volatile("s_memrealtime %0\n\ts_waitcnt lgkmcnt(0)" : "=s"(t_) :: "memory"); return t_; }
#define STAMP(i) do { if ((i) == PROBE % 100 && (w >= 4) == (PROBE >= 100)) { sbar(); tp_ = rtnow(); sbar(); } } while (0)
#define STAMP_INIT u64 t0_ = rtnow(), tp_ = t0_
#define STAMP_OUT do { while (rtnow() - tp_ < 3000) __builtin_amdgcn_s_sleep(2); } while (0)

constexpr int fV(int F) { return F < 80 ? F >> 4 : 5 + ((F - 80) % 12) / 4; }
constexpr int fK(int F) { return F < 80 ? (F >> 3) & 1 : (((F - 80) % 12) >> 1) & 1; }
constexpr int fJ(int F) { return F < 80 ? F & 7 : 2 * ((F - 80) / 12) + (F & 1); }

__global__ __launch_bounds__(512, 2) void k_conv_module(const char* x, const char* w1,
                                                        const char* dw, const char* w2,
                                                        char* ws, char* out) {
  u64x4 ka;
  u64 kb;
  asm volatile("s_load_dwordx8 %0, %2, 0x0\n\ts_load_dwordx2 %1, %2, 0x20" : "=&s"(ka), "=&s"(kb) : "s"(__builtin_amdgcn_kernarg_segment_ptr()));
  sbar();
  const int tid = threadIdx.x, lane = tid & 63;
  const int w = __builtin_amdgcn_readfirstlane(tid >> 6);
  STAMP_INIT;
  STAMP(0);

  const int u = w & 3;
  const int b = blockIdx.x, xcd = b & 7, idx = b >> 3, gidx = idx >> 2, mem = idx & 3;
  const int grp = xcd * 8 + gidx, batch = grp >> 5, tile = grp & 31;
  const int t0 = tile * 125;
  const int k0 = ROT ? ((2 * mem + gidx) & 7) : 0;
  const int l15 = lane & 15, q = lane >> 4;
  if (w >= 4) {
    __builtin_amdgcn_s_setprio(3);
    u32 lb = u * 1024;
    u32 xo[5];
    xo[0] = (u32)((t0 - 8 + 8 * u + (lane >> 3)) * 1024 + (((lane & 7) ^ ((4 * u + q) & 7)) << 4));
#pragma unroll
    for (int i = 1; i < 5; ++i) xo[i] = xo[0] + i * 32768;
    if (u >= 2) xo[4] = 0x80000000u;
    const u32 swz = (u32)(mem * 256 + ((l15 ^ ((q << 2) | (u & 2))) << 4));
    u32 woff0 = (u32)((4 * u + q) * 2048) + swz;
    u32 voff0 = (u32)((4 * u + q) * 1024) + swz;
    u32 doff = (u32)((lane >> 2) * 1024 + mem * 256 + ((lane >> 1) & 1) * 128 + u * 32 + (lane & 1) * 16);
    u32 x5 = u < 2 ? lb + XO + 16384 : DUMMY, x5s = u < 2 ? SL : 0, xb = batch * 4096000;
    PINS(lb); PINS(x5); PINS(x5s); PINS(xb); PINV(woff0); PINV(voff0); PINV(doff);
#pragma unroll
    for (int i = 0; i < 5; ++i) PINV(xo[i]);
    sbar();
    asm volatile("s_waitcnt lgkmcnt(0)\n\ts_mov_b64 %0, %5\n\ts_mov_b64 %1, %6\n\ts_mov_b64 %2, %7\n\ts_mov_b64 %3, %8\n\ts_mov_b64 %4, %9"
                 : "=&s"(x), "=&s"(w1), "=&s"(dw), "=&s"(w2), "=&s"(ws) : "s"(ka[0]), "s"(ka[1]), "s"(ka[2]), "s"(ka[3]), "s"(kb));
    sbar();
    char* gbase = ws + (size_t)grp * GSTR;
    char* flags = gbase + 131072;
    const __amdgpu_buffer_rsrc_t rx = __builtin_amdgcn_make_buffer_rsrc((void*)(x + xb), 0, 4096000, 0x00020000);
    const __amdgpu_buffer_rsrc_t rw = __builtin_amdgcn_make_buffer_rsrc((void*)w1, 0, 1048576, 0x00020000);
    const __amdgpu_buffer_rsrc_t rd = __builtin_amdgcn_make_buffer_rsrc((void*)dw, 0, 15360, 0x00020000);
    const __amdgpu_buffer_rsrc_t rv = __builtin_amdgcn_make_buffer_rsrc((void*)w2, 0, 524288, 0x00020000);
    auto xunit = [&](auto Pc) {
      constexpr int P = Pc.value, d = XO + (P % 3) * SL, sc = ROT ? 0 : P * 128;
      const u32 so = ROT ? ((k0 + P) & 7) * 128 : 0;
      glds<d, sc>(rx, lb, xo[0], so);
      glds<d + 4096, sc>(rx, lb, xo[1], so);
      glds<d + 8192, sc>(rx, lb, xo[2], so);
      glds<d + 12288, sc>(rx, lb, xo[3], so);
      glds<0, sc>(rx, x5 + (P % 3) * x5s, xo[4], so);
    };
    auto wunit = [&](auto Pc, auto kc) {
      constexpr int P = Pc.value, ks = kc.value, d = (P % 3) * SL + ks * 16384, sc = (ROT ? 0 : P * 131072) + ks * 65536;
      const u32 so = ROT ? ((k0 + P) & 7) * 131072 : 0;
      glds<d, sc>(rw, lb, woff0, so);
      glds<d + 8192, sc + 1024>(rw, lb, woff0, so);
      glds<d + 4096, sc + 32768>(rw, lb, woff0, so);
      glds<d + 12288, sc + 32768 + 1024>(rw, lb, woff0, so);
    };
    auto punit = [&](auto Pc) { xunit(Pc); wunit(Pc, IC<0>{}); wunit(Pc, IC<1>{}); };
    auto vunit = [&](auto nc) {
      constexpr int n = nc.value;
      const u32 c = n < 2 ? 2 * mem : 2 * ((mem + 1 + (n - 2) % 3) & 3);
      constexpr int o = (n < 2 ? n : n >= 5) * 65536;
      glds<VOFF[n], o>(rv, lb, voff0, c * 65536);
      glds<VOFF[n] + 4096, o + 16384>(rv, lb, voff0, c * 65536);
      glds<VOFF[n] + 8192, o + 32768>(rv, lb, voff0, c * 65536);
      glds<VOFF[n] + 12288, o + 49152>(rv, lb, voff0, c * 65536);
    };
    char* gdst = gbase + (mem * 8 + u) * 1024;
    auto poison = [&](int blk) {
#pragma unroll
      for (int r = 0; r < 4; ++r) gst(lane * 16, u32x4{~0u, ~0u, ~0u, ~0u}, gdst + r * 32768 + blk * 4096);
    };
    u32x4 T[2][2][8];
    auto tbuild = [&](int blk, int hi) {
      u32x4 v[8];
#pragma unroll
      for (int j = 0; j < 8; ++j) {
        const int jj = j ^ (hi << 2);
        u32 i = (u32)(8 * q + jj - (l15 & 7) - 1);
        if ((l15 >> 3) != hi) i = 15;
        v[j] = *LP(const u32x4, DWOFF + lb + (min(i, 15u) << 6) + blk * 32 + hi * 16);
      }
#pragma unroll
      for (int c = 0; c < 8; ++c)
#pragma unroll
        for (int m = 0; m < 4; ++m)
          T[blk][hi][c][m] = __builtin_amdgcn_perm(v[2 * m + 1][c >> 1], v[2 * m][c >> 1], (c & 1) ? 0x07060302u : 0x05040100u);
#pragma unroll
      for (int c = 0; c < 8; ++c) asm volatile("" : "+v"(T[blk][hi][c]));
    };
    punit(IC<0>{});
    glds<DWOFF, 0>(rd, lb, doff, 0);
    punit(IC<1>{});
    __builtin_amdgcn_s_setprio(0);
    STAMP(20);
    waitv<18>(); bar();
    STAMP(1);
    xunit(IC<2>{}); waitv<19>(); bar();
    STAMP(14);
    wunit(IC<2>{}, IC<0>{}); wunit(IC<2>{}, IC<1>{}); waitv<17>(); bar();
    STAMP(15);
    xunit(IC<3>{}); waitv<18>(); bar();
    STAMP(16);
    wunit(IC<3>{}, IC<0>{}); wunit(IC<3>{}, IC<1>{}); waitv<13>(); bar();
    STAMP(2);
    u64 G;
    punit(IC<4>{}); poison(0);
    asm volatile("global_load_dwordx2 %0, %1, %2 offset:128 sc1" : "=&v"(G) : "v"(0), "s"(flags) : "memory");
    tbuild(0, 0); waitv<18>(); bar();
    STAMP(17);
    punit(IC<5>{}); poison(1); tbuild(0, 1); waitv<22>(); bar();
    STAMP(3);
    punit(IC<6>{}); tbuild(1, 0); waitv<17>(); bar();
    STAMP(18);
    PINV(G);
    punit(IC<7>{});
    u64 tag = (G ^ (G >> 29)) * 0x9E3779B97F4A7C15ull + 0x632BE59BD9B4E019ull;
    if (tag == G) ++tag;
    PINV(tag);
    tbuild(1, 1); waitv<13>(); bar();
    STAMP(4);
    if (u == 0 && lane == 0) asm volatile("global_store_dwordx2 %0, %1, %2 sc1" ::"v"(mem * 8), "v"(tag), "s"(flags) : "memory");
    vunit(IC<0>{}); vunit(IC<1>{}); waitv<8>(); bar();
    STAMP(19);
    vunit(IC<2>{}); vunit(IC<3>{}); bar();
    STAMP(5);
    vunit(IC<4>{}); vunit(IC<5>{}); vunit(IC<6>{});
    __builtin_amdgcn_s_setprio(1);
    u32 hr[2][2], hw[2];
    char* gd[2][4];
#pragma unroll
    for (int blk = 0; blk < 2; ++blk) {
      const u32 him = (blk ? H1 : H0) + u * (16 * HSTR);
      const int hoff = ((q == 3 ? 0 : q) + l15) * 16;
      hr[blk][0] = him + hoff; hr[blk][1] = him + (hoff ^ 16); hw[blk] = him + lane * 16;
      PINV(hr[blk][0]); PINV(hr[blk][1]); PINV(hw[blk]);
#pragma unroll
      for (int r = 0; r < 4; ++r) { gd[blk][r] = gdst + r * 32768 + blk * 4096; PINS(gd[blk][r]); }
    }
    u32 l16 = lane * 16;
    PINV(l16);
    waitv<20>();
#pragma unroll
    for (int blk = 0; blk < 2; ++blk) {
      sbar();
      bar();
      STAMP(6 + 3 * blk);
      bf8 wv[16];
#pragma unroll
      for (int c = 0; c < 8; ++c) {
        wv[c] = ld128(hr[blk][c >> 2] + c * HSTR);
        wv[c + 8] = ld128(hr[blk][c >> 2] + (c + 8) * HSTR);
      }
      sbar();
      f32x4 sv[8];
#pragma unroll
      for (int c = 0; c < 8; ++c) {
        f32x4 d = mfma(__builtin_bit_cast(bf8, T[blk][0][c]), wv[c], f32x4{0, 0, 0, 0});
        sv[c] = mfma(__builtin_bit_cast(bf8, T[blk][1][c]), wv[c + 8], d);
      }
#pragma unroll
      for (int c = 0; c < 8; ++c) {
        const f32x2 lo = gate2(sv[c].xy, sv[c].xy), hi = gate2(sv[c].zw, sv[c].zw);
        sv[c] = f32x4{lo[0], lo[1], hi[0], hi[1]};
      }
#pragma unroll
      for (int r = 0; r < 4; ++r) {
        const u32x4 d = u32x4{min(pk2(sv[0][r], sv[1][r]), ~1u), min(pk2(sv[2][r], sv[3][r]), ~1u),
                              min(pk2(sv[4][r], sv[5][r]), ~1u), min(pk2(sv[6][r], sv[7][r]), ~1u)};
        gst(l16, d, gd[blk][r]);
        *LP(u32x4, hw[blk] + r * 1024) = d;
      }
      STAMP(7 + 3 * blk);
      if (blk == 0) waitv<16>(); else waitv<8>();
      waitl0();
    }
    bar();
    STAMP(12);
    vunit(IC<7>{});
    waitv<0>();
    STAMP(13);
    bar();
    STAMP_OUT;
    return;
  }
  waitl0();
  const int qq = l15 >> 2, p = lane & 3;
  u32 trb = 256 * (8 * q + qq) + ((((p >> 1) ^ ((qq << 2) | ((q & 1) << 1)))) << 4) + 8 * (p & 1);
  u64 fl;
  char* gbase = ws + (size_t)grp * GSTR;
  char* flags = gbase + 131072;
  u32 va[8];
#pragma unroll
  for (int j = 0; j < 8; ++j) { va[j] = (trb ^ (j << 5)) + BH; PINV(va[j]); }
  u32 goff = (q >> 1) * 1024 + (q & 1) * 512 + l15 * 16;
  u32 gla = (q >> 1) * (16 * HSTR) + u * 1024 + (q & 1) * 512 + l15 * 16, gla0 = gla + H0;
  PINV(goff); PINV(gla); PINV(gla0);
  const char* gs[8];
#pragma unroll
  for (int n = 2; n < 8; ++n) { gs[n] = gbase + (u * 32 + ((mem + 1 + (n - 2) % 3) & 3) * 8 + (n >= 5) * 4) * 1024; PINS(gs[n]); }
  u32 ooff[2];
  ooff[0] = (l15 * 8 + u) * 1024 + (q & 1) * 32 + (q >> 1) * 16;
  ooff[1] = ooff[0] + 4096;
  PINV(ooff[0]); PINV(ooff[1]);
  __amdgpu_buffer_rsrc_t ro = __builtin_amdgcn_make_buffer_rsrc(out + ((size_t)batch * 4000 + t0) * 1024 + mem * 256, 0, 124 * 1024 + 256, 0x00020000);
  PINS(ro); PINS(flags);
  {
    u32 xa0 = XO + l15 * 128 + ((q ^ (l15 >> 1)) << 4), xa1 = xa0 ^ 64;
    u32 wa0 = trb ^ (u << 5), wa1 = wa0 ^ 128;
    f32x4 acc[9][4];
#pragma unroll
    for (int m = 0; m < 9; ++m)
#pragma unroll
      for (int n = 0; n < 4; ++n) { acc[m][n] = f32x4{0, 0, 0, 0}; PINV(acc[m][n]); }
    u32 hg = u * (16 * HSTR) + l15 * HSTR + ((((q >> 1) ^ ((l15 >> 2) & 1)) << 4) | (((q & 1) ^ (l15 >> 3)) << 3));
    u32 hg0 = hg + H0, fo = (lane & 3) * 8;
    PINV(hg); PINV(hg0); PINV(fo); PINV(xa0); PINV(xa1); PINV(wa0); PINV(wa1);
    bf8 xf[9];
    bf4 wf[2][4][2];
    __builtin_amdgcn_s_setprio(2);
    waitl0();
    bar();
    STAMP(1);
#pragma unroll
    for (int n = 0; n < 4; ++n) {
      wf[0][n][0] = ldtr((n >> 1 ? wa1 : wa0) + (n & 1) * 8192);
      wf[0][n][1] = ldtr((n >> 1 ? wa1 : wa0) + (n & 1) * 8192 + 1024);
      if (n == 0) xf[0] = ld128(xa0);
    }
#pragma unroll
    for (int m = 1; m < 8; ++m) xf[m] = ld128(xa0 + m * 2048);
    int sl = 0, dlt = 0;
#pragma unroll 1
    for (int P = 0; P < 8; ++P) {
#pragma unroll
      for (int ks = 0; ks < 2; ++ks) {
#pragma unroll
        for (int m = 0; m < 9; ++m) {
#pragma unroll
          for (int n = 0; n < 4; ++n) {
            sbar();
            const int s = m * 4 + n;
            if (s == 16) {
              if (ks == 0) { if (P < 2) { STAMP(P ? 4 : 2); bar(); } }
              else { waitl0(); STAMP(P == 0 ? 3 : P == 1 ? 8 : P == 2 ? 15 : P == 3 ? 21 : P == 4 ? 22 : 23); bar(); }
            }
            const bf8 xv = xf[m];
            const u32 xn = ks ? xa0 : xa1;
            const int wo = ks ? 0 : 16384;
            auto rw_ = [&](int n2, int t) { wf[ks ^ 1][n2][t] = ldtr((n2 >> 1 ? wa1 : wa0) + wo + (n2 & 1) * 8192 + t * 1024); };
            auto rx_ = [&](int m2) { xf[m2] = ld128(xn + m2 * 2048); };
            if (s == 1) xf[8] = ld128((ks ? xa1 : xa0) + 8 * 2048);
            if (ks == 1) {
              if (s == 2) { dlt = sl == 2 ? -2 * SL : SL; PINS(dlt); }
              if (s == 3) { sl = sl == 2 ? 0 : sl + 1; PINS(sl); }
              if (s == 5) { xa0 += dlt; asm volatile("" : "+v"(xa0)); }
              if (s == 6) { xa1 += dlt; asm volatile("" : "+v"(xa1)); }
              if (s == 9) { wa0 += dlt; asm volatile("" : "+v"(wa0)); }
              if (s == 10) { wa1 += dlt; asm volatile("" : "+v"(wa1)); }
            }
            if (s == 17) rw_(0, 0);
            if (s == 18) rw_(0, 1);
            if (s == 19) rx_(0);
            if (s == 20) rw_(1, 0);
            if (s == 21) rw_(1, 1);
            if (s == 22) rx_(1);
            if (s == 23) rw_(2, 0);
            if (s == 24) rw_(2, 1);
            if (s == 25) rx_(2);
            if (s == 26) rw_(3, 0);
            if (s == 27) rw_(3, 1);
            if (s == 28) rx_(3);
            if (s == 29) rx_(4);
            if (s == 30) rx_(5);
            if (s == 31) rx_(6);
            if (s == 33) rx_(7);
            sbar();
            acc[m][n] = mfma(xv, cat(wf[ks][n][0], wf[ks][n][1]), acc[m][n]);
          }
        }
      }
    }
    sbar();
    __builtin_amdgcn_s_setprio(0);
    STAMP(5);
    asm volatile("global_load_dwordx2 %0, %1, %2 sc1" : "=&v"(fl) : "v"(fo), "s"(flags) : "memory");
    sbar();
#pragma unroll
    for (int blk = 0; blk < 2; ++blk) {
      const u32 him = blk ? hg + H1 : hg0;
#pragma unroll
      for (int m = 0; m < 9; ++m) {
        const f32x4 a = acc[m][2 * blk], g = acc[m][2 * blk + 1];
        const f32x2 lo = gate2(a.xy, g.xy), hi = gate2(a.zw, g.zw);
        *LP(u32x2, him + m * 32) = u32x2{pk2(lo[0], lo[1]), pk2(hi[0], hi[1])};
      }
      STAMP(6 + 3 * blk);
      sbar();
      if (blk == 0) {
        waitl0();
        bar();
        sbar();
        STAMP(7);
      }
    }
  }
  waitv<0>();
  PINV(fl);
  auto alleq = [&]() {
    const u64 f0 = ((u64)__builtin_amdgcn_readfirstlane((u32)(fl >> 32)) << 32) | (u32)__builtin_amdgcn_readfirstlane((u32)fl);
    return __all(fl == f0);
  };
  if (__builtin_expect(!alleq(), 0)) {
#pragma unroll 1
    for (int it = 0; it < (1 << 20); ++it) {
      __builtin_amdgcn_s_sleep(1);
      asm volatile("global_load_dwordx2 %0, %1, %2 sc1\n\ts_waitcnt vmcnt(0)" : "=&v"(fl) : "v"((lane & 3) * 8), "s"(flags) : "memory");
      if (alleq()) break;
    }
  }
  if (w == 0 && mem == 0 && lane == 0) asm volatile("global_store_dwordx2 %0, %1, %2 offset:128 sc1" ::"v"(0), "v"(fl), "s"(flags) : "memory");
  f32x4 acc2[2][8];
  bf4 af[8][2];
  u32x4 g[8][4];
  auto gsrc = [&](int n) { return gs[n]; };
  auto rdaf = [&](int F, int t) {
    const int v = fV(F);
    af[F & 7][t] = ldtr(va[fJ(F)] + (VOFF[v] - (v >= 2 && v <= 4 ? BL : BH)) + fK(F) * 8192 + t * 1024);
  };
  u32 mx = 0;
  auto settle = [&](int n) {
    if (__builtin_expect(__any(mx == ~0u), 0)) {
#pragma unroll 1
      for (int it = 0; it < (1 << 18); ++it) {
        gld4(g[n], goff, gsrc(n));
        waitv<0>();
        asm volatile("" : "+v"(g[n][0]), "+v"(g[n][1]), "+v"(g[n][2]), "+v"(g[n][3]));
        u32 m2 = 0;
#pragma unroll
        for (int i = 0; i < 8; ++i) m2 = max3(m2, g[n][i >> 1][(i & 1) * 2], g[n][i >> 1][(i & 1) * 2 + 1]);
        if (!__any(m2 == ~0u)) break;
      }
    }
  };
#pragma unroll
  for (int F = 0; F < 7; ++F) { rdaf(F, 0); rdaf(F, 1); }
  u32x4 od[2];
  sbar();
  waitl0();
  bar();
  STAMP(10);
  sfor<0, 128>([&](auto Fc) {
    constexpr int F = Fc.value;
    constexpr int v = fV(F), j = fJ(F), kk = fK(F);
    sbar();
    if (F == 16) { STAMP(11); bar(); STAMP(12); }
    if (F == 80) { STAMP(16); bar(); STAMP(17); }
    if (F == 0 || F == 16) {
#pragma unroll
      for (int i = 0; i < 4; ++i) g[v][i] = __builtin_bit_cast(u32x4, ld128((v ? gla + H1 : gla0) + (i >> 1) * (32 * HSTR) + (i & 1) * 256));
    }
    if (F == G0AT) { gld4(g[2], goff, gsrc(2)); gld4(g[3], goff, gsrc(3)); gld4(g[4], goff, gsrc(4)); }
    if (F == G1AT) { gld4(g[5], goff, gsrc(5)); gld4(g[6], goff, gsrc(6)); gld4(g[7], goff, gsrc(7)); }
#pragma unroll
    for (int n = 2; n < 8; ++n) {
      const int c0 = n < 5 ? 16 * n - 4 : 76 + 4 * (n - 5);
      if (F == c0 + 4) { settle(n); if (n == 2) STAMP(14); }
    }
#pragma unroll
    for (int n = 2; n < 8; ++n) {
      const int c0 = n < 5 ? 16 * n - 4 : 76 + 4 * (n - 5);
      if (F == c0) {
        if (n == 2) STAMP(13);
        const int yng = n < 5 ? 4 * (4 - n) + (G1AT <= c0 ? 12 : 0) : 4 * (7 - n);
        if (yng == 20) waitv<20>(); if (yng == 16) waitv<16>(); if (yng == 12) waitv<12>();
        if (yng == 8) waitv<8>(); if (yng == 4) waitv<4>(); if (yng == 0) waitv<0>();
        asm volatile("" : "+v"(g[n][0]), "+v"(g[n][1]), "+v"(g[n][2]), "+v"(g[n][3]));
        mx = 0;
      }
    }
#pragma unroll
    for (int s = 0; s < 2; ++s) {
      sbar();
      if (F + 7 < 128) {
        const int Fn = F + 7;
        rdaf(Fn, s);
        if (s == 1 && Fn >= 24 && Fn < 32) { va[Fn & 7] += BL - BH; asm volatile("" : "+v"(va[Fn & 7])); }
        if (s == 1 && Fn >= 72 && Fn < 80) { va[Fn & 7] += BH - BL; asm volatile("" : "+v"(va[Fn & 7])); }
      }
#pragma unroll
      for (int n = 2; n < 8; ++n) {
        const int c0 = n < 5 ? 16 * n - 4 : 76 + 4 * (n - 5);
        if (F >= c0 && F < c0 + 4) { mx = max3(mx, g[n][F - c0][2 * s], g[n][F - c0][2 * s + 1]); asm volatile("" : "+v"(mx)); }
      }
      if constexpr (F >= 93) {
        constexpr int J = (F - 93) / 12;
        const int e = ((F - 93) % 12) * 2 + s;
        if (e < 16) {
          const int s2 = e >> 3, e2 = e & 7;
          if (e2 < 4) { const f32x4 a = acc2[s2][2 * J + (e2 >> 1)]; od[s2][e2] = pk2(a[2 * (e2 & 1)], a[2 * (e2 & 1) + 1]); asm volatile("" : "+v"(od[s2][e2])); }
          if (e2 == 5 || e2 == 6) { auto r = __builtin_amdgcn_permlane16_swap(od[s2][e2 - 5], od[s2][e2 - 3], false, false); od[s2][e2 - 5] = r[0]; od[s2][e2 - 3] = r[1]; }
          if (e2 == 7) gsto<J * 64>(ooff[s2], od[s2], ro);
        }
      }
      sbar();
      if (F < 8) mfma_new(acc2[s][j], cat(af[F & 7][0], af[F & 7][1]), g[v][kk * 2 + s]);
      else mfma_acc(acc2[s][j], cat(af[F & 7][0], af[F & 7][1]), g[v][kk * 2 + s]);
    }
  });
  sbar();
  asm volatile("s_nop 7\n\ts_nop 3");
  sbar();
  STAMP(18);
#pragma unroll
  for (int s2 = 0; s2 < 2; ++s2) {
#pragma unroll
    for (int e2 = 0; e2 < 4; ++e2) { const f32x4 a = acc2[s2][6 + (e2 >> 1)]; od[s2][e2] = pk2(a[2 * (e2 & 1)], a[2 * (e2 & 1) + 1]); }
#pragma unroll
    for (int e2 = 0; e2 < 2; ++e2) { auto r = __builtin_amdgcn_permlane16_swap(od[s2][e2], od[s2][e2 + 2], false, false); od[s2][e2] = r[0]; od[s2][e2 + 2] = r[1]; }
    gsto<192>(ooff[s2], od[s2], ro);
  }
  STAMP(19);
  waitv<0>();
  STAMP(20);
  STAMP_OUT;
}

extern "C" void kernel_launch(void* const* d_in, const int* in_sizes, int n_in, void* d_out, int out_size, void* d_ws,
                              size_t ws_size, hipStream_t stream) {
  (void)hipFuncSetAttribute((const void*)k_conv_module, hipFuncAttributeMaxDynamicSharedMemorySize, LDSZ);
  k_conv_module<<<256, 512, LDSZ, stream>>>((const char*)d_in[0], (const char*)d_in[1], (const char*)d_in[2], (const char*)d_in[3],
                                            (char*)d_ws, (char*)d_out);
}
```

```cpp
#define PROBE 0
#define PWG 0
#include <hip/hip_runtime.h>
#include <hip/hip_bf16.h>

typedef unsigned u32;
typedef unsigned long long u64;
typedef __attribute__((ext_vector_type(2))) unsigned u32x2;
typedef __attribute__((ext_vector_type(4))) unsigned u32x4;
typedef __attribute__((ext_vector_type(2))) unsigned long long u64x2;
typedef __attribute__((ext_vector_type(4))) unsigned long long u64x4;
typedef __attribute__((ext_vector_type(2))) __bf16 bf2;
typedef __attribute__((ext_vector_type(4))) __bf16 bf4;
typedef __attribute__((ext_vector_type(8))) __bf16 bf8;
typedef __attribute__((ext_vector_type(2))) float f32x2;
typedef __attribute__((ext_vector_type(4))) float f32x4;
#define LAS __attribute__((address_space(3)))
#define GAS __attribute__((address_space(1)))
#define DEVI __device__ __forceinline__
#define RLX_AGENT __ATOMIC_RELAXED, __HIP_MEMORY_SCOPE_AGENT

#define ROT 0
#define G0AT 12
#define G1AT 40

template <int N> DEVI void waitv() { asm volatile("s_waitcnt vmcnt(%0)" ::"n"(N) : "memory"); }
DEVI void sbar() { __builtin_amdgcn_sched_barrier(0); }
DEVI void waitl0() { sbar(); __builtin_amdgcn_s_waitcnt(0xC07F); sbar(); }
#define PINV(x) asm volatile("" : "+v"(x))
#define PINS(x) asm volatile("" : "+s"(x))
DEVI void bar() { asm volatile("s_barrier" ::: "memory"); }

DEVI u32 pk2(float a, float b) {
  f32x2 v = {a, b};
  bf2 r = __builtin_convertvector(v, bf2);
  return __builtin_bit_cast(u32, r);
}
DEVI f32x2 gate2(f32x2 a, f32x2 b) {
  const f32x2 t = b * -1.4426950408889634f;
  f32x2 e = {__builtin_amdgcn_exp2f(t[0]), __builtin_amdgcn_exp2f(t[1])};
  e = e + 1.0f;
  const f32x2 r = {__builtin_amdgcn_rcpf(e[0]), __builtin_amdgcn_rcpf(e[1])};
  return a * r;
}
#define LP(T, a) ((LAS T*)(size_t)(a))
DEVI bf8 ld128(u32 a) { return *LP(const bf8, a); }
DEVI bf4 ldtr(u32 a) { return __builtin_amdgcn_ds_read_tr16_b64_v4bf16(LP(bf4, a)); }
DEVI bf8 cat(bf4 a, bf4 b) { return __builtin_shufflevector(a, b, 0, 1, 2, 3, 4, 5, 6, 7); }
DEVI f32x4 mfma(bf8 a, bf8 b, f32x4 c) { return __builtin_amdgcn_mfma_f32_16x16x32_bf16(a, b, c, 0, 0, 0); }
template <int D, int S> DEVI void glds(__amdgpu_buffer_rsrc_t r, u32 dst, u32 voff, u32 soff) {
  u32 t;
  asm volatile("s_add_u32 m0, %1, %5\n\ts_add_u32 %0, %4, %6\n\tbuffer_load_dwordx4 %2, %3, %0 offen lds"
               : "=&s"(t) : "s"(dst), "v"(voff), "s"(r), "s"(soff), "n"(D), "n"(S) : "memory", "scc");
}
DEVI void gst(u32 voff, u32x4 d, char* base) {
  asm volatile("global_store_dwordx4 %0, %1, %2 sc1\n\ts_nop 1" ::"v"(voff), "v"(d), "s"(base) : "memory");
}
template <int O> DEVI void gsto(u32 voff, u32x4 d, __amdgpu_buffer_rsrc_t r) {
  asm volatile("buffer_store_dwordx4 %1, %0, %2, 0 offen offset:%3 sc1" ::"v"(voff), "v"(d), "s"(r), "n"(O) : "memory");
}
DEVI void mfma_acc(f32x4& c, bf8 a, u32x4 b) { asm volatile("v_mfma_f32_16x16x32_bf16 %0, %1, %2, %0" : "+v"(c) : "v"(a), "v"(b)); }
DEVI void mfma_new(f32x4& c, bf8 a, u32x4 b) { asm volatile("v_mfma_f32_16x16x32_bf16 %0, %1, %2, 0" : "=&v"(c) : "v"(a), "v"(b)); }
DEVI void gld4(u32x4* g, u32 voff, const char* base) {
  asm volatile("global_load_dwordx4 %0, %4, %5 sc1\n\tglobal_load_dwordx4 %1, %4, %5 offset:256 sc1\n\t"
               "global_load_dwordx4 %2, %4, %5 offset:2048 sc1\n\tglobal_load_dwordx4 %3, %4, %5 offset:2304 sc1"
               : "=&v"(g[0]), "=&v"(g[1]), "=&v"(g[2]), "=&v"(g[3])
               : "v"(voff), "s"(base) : "memory");
}
template <int V> struct IC { static constexpr int value = V; };
template <int I, int N, class Fn> DEVI void sfor(Fn&& f) {
  if constexpr (I < N) { f(IC<I>{}); sfor<I + 1, N>(f); }
}
DEVI u32 max3(u32 a, u32 b, u32 c) { return max(max(a, b), c); }

constexpr int SL = 51200;
constexpr int XO = 32768;
constexpr int DWOFF = 3 * SL;
constexpr int DUMMY = DWOFF + 4096;
constexpr int CTL = DWOFF + 5120;
constexpr int LDSZ = 163840;
constexpr int HSTR = 288;
constexpr int H0 = 2 * SL + XO, H1 = 0;
constexpr int BH = 69632, BL = 18432;
constexpr int VOFF[8] = {BH + 32768, BH + 49152, BL, BL + 16384, BL + 34816, BH, BH + 16384, BH + 32768};
constexpr int GSTR = 131072 + 256;

DEVI u64 rtnow() { u64 t_; asm volatile("s_memrealtime %0\n\ts_waitcnt lgkmcnt(0)" : "=s"(t_) :: "memory"); return t_; }
DEVI u64 cynow() { u64 t_; asm volatile("s_memtime %0\n\ts_waitcnt lgkmcnt(0)" : "=s"(t_) :: "memory"); return t_; }
#define STAMP(i) do { if ((i) == PROBE % 100 && (i) != 0 && (w >= 4) == (PROBE >= 100)) { sbar(); tp_ = rtnow(); sbar(); } } while (0)
#define STAMP_INIT
#define STAMP_PRE u64 t0_, c0_, tp_; asm volatile("s_memrealtime %0\n\ts_memtime %1" : "=s"(t0_), "=s"(c0_) :: "memory")
#define STAMP_POST do { PINS(t0_); PINS(c0_); tp_ = t0_; } while (0)
#define STAMP_OUT do { if (PWG < 0 || blockIdx.x == PWG) while ((long long)(rtnow() - tp_) < 3000) __builtin_amdgcn_s_sleep(2); } while (0)

constexpr int fV(int F) { return F < 80 ? F >> 4 : 5 + ((F - 80) % 12) / 4; }
constexpr int fK(int F) { return F < 80 ? (F >> 3) & 1 : (((F - 80) % 12) >> 1) & 1; }
constexpr int fJ(int F) { return F < 80 ? F & 7 : 2 * ((F - 80) / 12) + (F & 1); }

__global__ __launch_bounds__(512, 2) void k_conv_module(const char* x, const char* w1,
                                                        const char* dw, const char* w2,
                                                        char* ws, char* out) {
  STAMP_PRE;
  u64x4 ka;
  u64 kb;
  asm volatile("s_load_dwordx8 %0, %2, 0x0\n\ts_load_dwordx2 %1, %2, 0x20" : "=&s"(ka), "=&s"(kb) : "s"(__builtin_amdgcn_kernarg_segment_ptr()));
  sbar();
  const int tid = threadIdx.x, lane = tid & 63;
  const int w = __builtin_amdgcn_readfirstlane(tid >> 6);
  STAMP_INIT;
  STAMP(0);

  const int u = w & 3;
  const int b = blockIdx.x, xcd = b & 7, idx = b >> 3, gidx = idx >> 2, mem = idx & 3;
  const int grp = xcd * 8 + gidx, batch = grp >> 5, tile = grp & 31;
  const int t0 = tile * 125;
  const int k0 = ROT ? ((2 * mem + gidx) & 7) : 0;
  const int l15 = lane & 15, q = lane >> 4;
  if (w >= 4) {
    __builtin_amdgcn_s_setprio(3);
    u32 lb = u * 1024;
    u32 xo[5];
    xo[0] = (u32)((t0 - 8 + 8 * u + (lane >> 3)) * 1024 + (((lane & 7) ^ ((4 * u + q) & 7)) << 4));
#pragma unroll
    for (int i = 1; i < 5; ++i) xo[i] = xo[0] + i * 32768;
    if (u >= 2) xo[4] = 0x80000000u;
    const u32 swz = (u32)(mem * 256 + ((l15 ^ ((q << 2) | (u & 2))) << 4));
    u32 woff0 = (u32)((4 * u + q) * 2048) + swz;
    u32 voff0 = (u32)((4 * u + q) * 1024) + swz;
    u32 doff = (u32)((lane >> 2) * 1024 + mem * 256 + ((lane >> 1) & 1) * 128 + u * 32 + (lane & 1) * 16);
    u32 x5 = u < 2 ? lb + XO + 16384 : DUMMY, x5s = u < 2 ? SL : 0, xb = batch * 4096000;
    PINS(lb); PINS(x5); PINS(x5s); PINS(xb); PINV(woff0); PINV(voff0); PINV(doff);
#pragma unroll
    for (int i = 0; i < 5; ++i) PINV(xo[i]);
    sbar();
    asm volatile("s_waitcnt lgkmcnt(0)\n\ts_mov_b64 %0, %5\n\ts_mov_b64 %1, %6\n\ts_mov_b64 %2, %7\n\ts_mov_b64 %3, %8\n\ts_mov_b64 %4, %9"
                 : "=&s"(x), "=&s"(w1), "=&s"(dw), "=&s"(w2), "=&s"(ws) : "s"(ka[0]), "s"(ka[1]), "s"(ka[2]), "s"(ka[3]), "s"(kb));
    STAMP_POST;
    sbar();
    char* gbase = ws + (size_t)grp * GSTR;
    char* flags = gbase + 131072;
    const __amdgpu_buffer_rsrc_t rx = __builtin_amdgcn_make_buffer_rsrc((void*)(x + xb), 0, 4096000, 0x00020000);
    const __amdgpu_buffer_rsrc_t rw = __builtin_amdgcn_make_buffer_rsrc((void*)w1, 0, 1048576, 0x00020000);
    const __amdgpu_buffer_rsrc_t rd = __builtin_amdgcn_make_buffer_rsrc((void*)dw, 0, 15360, 0x00020000);
    const __amdgpu_buffer_rsrc_t rv = __builtin_amdgcn_make_buffer_rsrc((void*)w2, 0, 524288, 0x00020000);
    auto xunit = [&](auto Pc) {
      constexpr int P = Pc.value, d = XO + (P % 3) * SL, sc = ROT ? 0 : P * 128;
      const u32 so = ROT ? ((k0 + P) & 7) * 128 : 0;
      glds<d, sc>(rx, lb, xo[0], so);
      glds<d + 4096, sc>(rx, lb, xo[1], so);
      glds<d + 8192, sc>(rx, lb, xo[2], so);
      glds<d + 12288, sc>(rx, lb, xo[3], so);
      glds<0, sc>(rx, x5 + (P % 3) * x5s, xo[4], so);
    };
    auto wunit = [&](auto Pc, auto kc) {
      constexpr int P = Pc.value, ks = kc.value, d = (P % 3) * SL + ks * 16384, sc = (ROT ? 0 : P * 131072) + ks * 65536;
      const u32 so = ROT ? ((k0 + P) & 7) * 131072 : 0;
      glds<d, sc>(rw, lb, woff0, so);
      glds<d + 8192, sc + 1024>(rw, lb, woff0, so);
      glds<d + 4096, sc + 32768>(rw, lb, woff0, so);
      glds<d + 12288, sc + 32768 + 1024>(rw, lb, woff0, so);
    };
    auto punit = [&](auto Pc) { xunit(Pc); wunit(Pc, IC<0>{}); wunit(Pc, IC<1>{}); };
    auto vunit = [&](auto nc) {
      constexpr int n = nc.value;
      const u32 c = n < 2 ? 2 * mem : 2 * ((mem + 1 + (n - 2) % 3) & 3);
      constexpr int o = (n < 2 ? n : n >= 5) * 65536;
      glds<VOFF[n], o>(rv, lb, voff0, c * 65536);
      glds<VOFF[n] + 4096, o + 16384>(rv, lb, voff0, c * 65536);
      glds<VOFF[n] + 8192, o + 32768>(rv, lb, voff0, c * 65536);
      glds<VOFF[n] + 12288, o + 49152>(rv, lb, voff0, c * 65536);
    };
    char* gdst = gbase + (mem * 8 + u) * 1024;
    auto poison = [&](int blk) {
#pragma unroll
      for (int r = 0; r < 4; ++r) gst(lane * 16, u32x4{~0u, ~0u, ~0u, ~0u}, gdst + r * 32768 + blk * 4096);
    };
    u32x4 T[2][2][8];
    auto tbuild = [&](int blk, int hi) {
      u32x4 v[8];
#pragma unroll
      for (int j = 0; j < 8; ++j) {
        const int jj = j ^ (hi << 2);
        u32 i = (u32)(8 * q + jj - (l15 & 7) - 1);
        if ((l15 >> 3) != hi) i = 15;
        v[j] = *LP(const u32x4, DWOFF + lb + (min(i, 15u) << 6) + blk * 32 + hi * 16);
      }
#pragma unroll
      for (int c = 0; c < 8; ++c)
#pragma unroll
        for (int m = 0; m < 4; ++m)
          T[blk][hi][c][m] = __builtin_amdgcn_perm(v[2 * m + 1][c >> 1], v[2 * m][c >> 1], (c & 1) ? 0x07060302u : 0x05040100u);
#pragma unroll
      for (int c = 0; c < 8; ++c) asm volatile("" : "+v"(T[blk][hi][c]));
    };
    punit(IC<0>{});
    glds<DWOFF, 0>(rd, lb, doff, 0);
    punit(IC<1>{});
    __builtin_amdgcn_s_setprio(0);
    STAMP(20);
    waitv<18>(); bar();
    STAMP(1);
    xunit(IC<2>{}); waitv<19>(); bar();
    STAMP(14);
    wunit(IC<2>{}, IC<0>{}); wunit(IC<2>{}, IC<1>{}); waitv<17>(); bar();
    STAMP(15);
    xunit(IC<3>{}); waitv<18>(); bar();
    STAMP(16);
    wunit(IC<3>{}, IC<0>{}); wunit(IC<3>{}, IC<1>{}); waitv<13>(); bar();
    STAMP(2);
    u64 G;
    punit(IC<4>{}); poison(0);
    asm volatile("global_load_dwordx2 %0, %1, %2 offset:128 sc1" : "=&v"(G) : "v"(0), "s"(flags) : "memory");
    tbuild(0, 0); waitv<18>(); bar();
    STAMP(17);
    punit(IC<5>{}); poison(1); tbuild(0, 1); waitv<22>(); bar();
    STAMP(3);
    punit(IC<6>{}); tbuild(1, 0); waitv<17>(); bar();
    STAMP(18);
    PINV(G);
    punit(IC<7>{});
    u64 tag = (G ^ (G >> 29)) * 0x9E3779B97F4A7C15ull + 0x632BE59BD9B4E019ull;
    if (tag == G) ++tag;
    PINV(tag);
    tbuild(1, 1); waitv<13>(); bar();
    STAMP(4);
    if (u == 0 && lane == 0) asm volatile("global_store_dwordx2 %0, %1, %2 sc1" ::"v"(mem * 8), "v"(tag), "s"(flags) : "memory");
    vunit(IC<0>{}); vunit(IC<1>{}); waitv<8>(); bar();
    STAMP(19);
    vunit(IC<2>{}); vunit(IC<3>{}); bar();
    STAMP(5);
    vunit(IC<4>{}); vunit(IC<5>{}); vunit(IC<6>{});
    __builtin_amdgcn_s_setprio(1);
    u32 hr[2][2], hw[2];
    char* gd[2][4];
#pragma unroll
    for (int blk = 0; blk < 2; ++blk) {
      const u32 him = (blk ? H1 : H0) + u * (16 * HSTR);
      const int hoff = ((q == 3 ? 0 : q) + l15) * 16;
      hr[blk][0] = him + hoff; hr[blk][1] = him + (hoff ^ 16); hw[blk] = him + lane * 16;
      PINV(hr[blk][0]); PINV(hr[blk][1]); PINV(hw[blk]);
#pragma unroll
      for (int r = 0; r < 4; ++r) { gd[blk][r] = gdst + r * 32768 + blk * 4096; PINS(gd[blk][r]); }
    }
    u32 l16 = lane * 16;
    PINV(l16);
    waitv<20>();
#pragma unroll
    for (int blk = 0; blk < 2; ++blk) {
      sbar();
      bar();
      STAMP(6 + 3 * blk);
      bf8 wv[16];
#pragma unroll
      for (int c = 0; c < 8; ++c) {
        wv[c] = ld128(hr[blk][c >> 2] + c * HSTR);
        wv[c + 8] = ld128(hr[blk][c >> 2] + (c + 8) * HSTR);
      }
      sbar();
      f32x4 sv[8];
#pragma unroll
      for (int c = 0; c < 8; ++c) {
        f32x4 d = mfma(__builtin_bit_cast(bf8, T[blk][0][c]), wv[c], f32x4{0, 0, 0, 0});
        sv[c] = mfma(__builtin_bit_cast(bf8, T[blk][1][c]), wv[c + 8], d);
      }
#pragma unroll
      for (int c = 0; c < 8; ++c) {
        const f32x2 lo = gate2(sv[c].xy, sv[c].xy), hi = gate2(sv[c].zw, sv[c].zw);
        sv[c] = f32x4{lo[0], lo[1], hi[0], hi[1]};
      }
#pragma unroll
      for (int r = 0; r < 4; ++r) {
        const u32x4 d = u32x4{min(pk2(sv[0][r], sv[1][r]), ~1u), min(pk2(sv[2][r], sv[3][r]), ~1u),
                              min(pk2(sv[4][r], sv[5][r]), ~1u), min(pk2(sv[6][r], sv[7][r]), ~1u)};
        gst(l16, d, gd[blk][r]);
        *LP(u32x4, hw[blk] + r * 1024) = d;
      }
      STAMP(7 + 3 * blk);
      if (blk == 0) waitv<16>(); else waitv<8>();
      waitl0();
    }
    bar();
    STAMP(12);
    vunit(IC<7>{});
    waitv<0>();
    STAMP(13);
    bar();
    STAMP_OUT;
    return;
  }
  waitl0();
  STAMP_POST;
  const int qq = l15 >> 2, p = lane & 3;
  u32 trb = 256 * (8 * q + qq) + ((((p >> 1) ^ ((qq << 2) | ((q & 1) << 1)))) << 4) + 8 * (p & 1);
  u64 fl;
  char* gbase = ws + (size_t)grp * GSTR;
  char* flags = gbase + 131072;
  u32 va[8];
#pragma unroll
  for (int j = 0; j < 8; ++j) { va[j] = (trb ^ (j << 5)) + BH; PINV(va[j]); }
  u32 goff = (q >> 1) * 1024 + (q & 1) * 512 + l15 * 16;
  u32 gla = (q >> 1) * (16 * HSTR) + u * 1024 + (q & 1) * 512 + l15 * 16, gla0 = gla + H0;
  PINV(goff); PINV(gla); PINV(gla0);
  const char* gs[8];
#pragma unroll
  for (int n = 2; n < 8; ++n) { gs[n] = gbase + (u * 32 + ((mem + 1 + (n - 2) % 3) & 3) * 8 + (n >= 5) * 4) * 1024; PINS(gs[n]); }
  u32 ooff[2];
  ooff[0] = (l15 * 8 + u) * 1024 + (q & 1) * 32 + (q >> 1) * 16;
  ooff[1] = ooff[0] + 4096;
  PINV(ooff[0]); PINV(ooff[1]);
  __amdgpu_buffer_rsrc_t ro = __builtin_amdgcn_make_buffer_rsrc(out + ((size_t)batch * 4000 + t0) * 1024 + mem * 256, 0, 124 * 1024 + 256, 0x00020000);
  PINS(ro); PINS(flags);
  {
    u32 xa0 = XO + l15 * 128 + ((q ^ (l15 >> 1)) << 4), xa1 = xa0 ^ 64;
    u32 wa0 = trb ^ (u << 5), wa1 = wa0 ^ 128;
    f32x4 acc[9][4];
#pragma unroll
    for (int m = 0; m < 9; ++m)
#pragma unroll
      for (int n = 0; n < 4; ++n) { acc[m][n] = f32x4{0, 0, 0, 0}; PINV(acc[m][n]); }
    u32 hg = u * (16 * HSTR) + l15 * HSTR + ((((q >> 1) ^ ((l15 >> 2) & 1)) << 4) | (((q & 1) ^ (l15 >> 3)) << 3));
    u32 hg0 = hg + H0, fo = (lane & 3) * 8;
    PINV(hg); PINV(hg0); PINV(fo); PINV(xa0); PINV(xa1); PINV(wa0); PINV(wa1);
    bf8 xf[9];
    bf4 wf[2][4][2];
    __builtin_amdgcn_s_setprio(2);
    waitl0();
    bar();
    STAMP(1);
#pragma unroll
    for (int n = 0; n < 4; ++n) {
      wf[0][n][0] = ldtr((n >> 1 ? wa1 : wa0) + (n & 1) * 8192);
      wf[0][n][1] = ldtr((n >> 1 ? wa1 : wa0) + (n & 1) * 8192 + 1024);
      if (n == 0) xf[0] = ld128(xa0);
    }
#pragma unroll
    for (int m = 1; m < 8; ++m) xf[m] = ld128(xa0 + m * 2048);
    int sl = 0, dlt = 0;
#pragma unroll 1
    for (int P = 0; P < 8; ++P) {
#pragma unroll
      for (int ks = 0; ks < 2; ++ks) {
#pragma unroll
        for (int m = 0; m < 9; ++m) {
#pragma unroll
          for (int n = 0; n < 4; ++n) {
            sbar();
            const int s = m * 4 + n;
            if (s == 16) {
              if (ks == 0) { if (P < 2) { STAMP(P ? 4 : 2); bar(); } }
              else { waitl0(); STAMP(P == 0 ? 3 : P == 1 ? 8 : P == 2 ? 15 : P == 3 ? 21 : P == 4 ? 22 : 23); bar(); }
            }
            const bf8 xv = xf[m];
            const u32 xn = ks ? xa0 : xa1;
            const int wo = ks ? 0 : 16384;
            auto rw_ = [&](int n2, int t) { wf[ks ^ 1][n2][t] = ldtr((n2 >> 1 ? wa1 : wa0) + wo + (n2 & 1) * 8192 + t * 1024); };
            auto rx_ = [&](int m2) { xf[m2] = ld128(xn + m2 * 2048); };
            if (s == 1) xf[8] = ld128((ks ? xa1 : xa0) + 8 * 2048);
            if (ks == 1) {
              if (s == 2) { dlt = sl == 2 ? -2 * SL : SL; PINS(dlt); }
              if (s == 3) { sl = sl == 2 ? 0 : sl + 1; PINS(sl); }
              if (s == 5) { xa0 += dlt; asm volatile("" : "+v"(xa0)); }
              if (s == 6) { xa1 += dlt; asm volatile("" : "+v"(xa1)); }
              if (s == 9) { wa0 += dlt; asm volatile("" : "+v"(wa0)); }
              if (s == 10) { wa1 += dlt; asm volatile("" : "+v"(wa1)); }
            }
            if (s == 17) rw_(0, 0);
            if (s == 18) rw_(0, 1);
            if (s == 19) rx_(0);
            if (s == 20) rw_(1, 0);
            if (s == 21) rw_(1, 1);
            if (s == 22) rx_(1);
            if (s == 23) rw_(2, 0);
            if (s == 24) rw_(2, 1);
            if (s == 25) rx_(2);
            if (s == 26) rw_(3, 0);
            if (s == 27) rw_(3, 1);
            if (s == 28) rx_(3);
            if (s == 29) rx_(4);
            if (s == 30) rx_(5);
            if (s == 31) rx_(6);
            if (s == 33) rx_(7);
            sbar();
            acc[m][n] = mfma(xv, cat(wf[ks][n][0], wf[ks][n][1]), acc[m][n]);
          }
        }
      }
    }
    sbar();
    __builtin_amdgcn_s_setprio(0);
    STAMP(5);
    asm volatile("global_load_dwordx2 %0, %1, %2 sc1" : "=&v"(fl) : "v"(fo), "s"(flags) : "memory");
    sbar();
#pragma unroll
    for (int blk = 0; blk < 2; ++blk) {
      const u32 him = blk ? hg + H1 : hg0;
#pragma unroll
      for (int m = 0; m < 9; ++m) {
        const f32x4 a = acc[m][2 * blk], g = acc[m][2 * blk + 1];
        const f32x2 lo = gate2(a.xy, g.xy), hi = gate2(a.zw, g.zw);
        *LP(u32x2, him + m * 32) = u32x2{pk2(lo[0], lo[1]), pk2(hi[0], hi[1])};
      }
      STAMP(6 + 3 * blk);
      sbar();
      if (blk == 0) {
        waitl0();
        bar();
        sbar();
        STAMP(7);
      }
    }
  }
  waitv<0>();
  PINV(fl);
  auto alleq = [&]() {
    const u64 f0 = ((u64)__builtin_amdgcn_readfirstlane((u32)(fl >> 32)) << 32) | (u32)__builtin_amdgcn_readfirstlane((u32)fl);
    return __all(fl == f0);
  };
  if (__builtin_expect(!alleq(), 0)) {
#pragma unroll 1
    for (int it = 0; it < (1 << 20); ++it) {
      __builtin_amdgcn_s_sleep(1);
      asm volatile("global_load_dwordx2 %0, %1, %2 sc1\n\ts_waitcnt vmcnt(0)" : "=&v"(fl) : "v"((lane & 3) * 8), "s"(flags) : "memory");
      if (alleq()) break;
    }
  }
  if (w == 0 && mem == 0 && lane == 0) asm volatile("global_store_dwordx2 %0, %1, %2 offset:128 sc1" ::"v"(0), "v"(fl), "s"(flags) : "memory");
  f32x4 acc2[2][8];
  bf4 af[8][2];
  u32x4 g[8][4];
  auto gsrc = [&](int n) { return gs[n]; };
  auto rdaf = [&](int F, int t) {
    const int v = fV(F);
    af[F & 7][t] = ldtr(va[fJ(F)] + (VOFF[v] - (v >= 2 && v <= 4 ? BL : BH)) + fK(F) * 8192 + t * 1024);
  };
  u32 mx = 0;
  auto settle = [&](int n) {
    if (__builtin_expect(__any(mx == ~0u), 0)) {
#pragma unroll 1
      for (int it = 0; it < (1 << 18); ++it) {
        gld4(g[n], goff, gsrc(n));
        waitv<0>();
        asm volatile("" : "+v"(g[n][0]), "+v"(g[n][1]), "+v"(g[n][2]), "+v"(g[n][3]));
        u32 m2 = 0;
#pragma unroll
        for (int i = 0; i < 8; ++i) m2 = max3(m2, g[n][i >> 1][(i & 1) * 2], g[n][i >> 1][(i & 1) * 2 + 1]);
        if (!__any(m2 == ~0u)) break;
      }
    }
  };
#pragma unroll
  for (int F = 0; F < 7; ++F) { rdaf(F, 0); rdaf(F, 1); }
  u32x4 od[2];
  sbar();
  waitl0();
  bar();
  STAMP(10);
  sfor<0, 128>([&](auto Fc) {
    constexpr int F = Fc.value;
    constexpr int v = fV(F), j = fJ(F), kk = fK(F);
    sbar();
    if (F == 16) { STAMP(11); bar(); STAMP(12); }
    if (F == 80) { STAMP(16); bar(); STAMP(17); }
    if (F == 0 || F == 16) {
#pragma unroll
      for (int i = 0; i < 4; ++i) g[v][i] = __builtin_bit_cast(u32x4, ld128((v ? gla + H1 : gla0) + (i >> 1) * (32 * HSTR) + (i & 1) * 256));
    }
    if (F == G0AT) { gld4(g[2], goff, gsrc(2)); gld4(g[3], goff, gsrc(3)); gld4(g[4], goff, gsrc(4)); }
    if (F == G1AT) { gld4(g[5], goff, gsrc(5)); gld4(g[6], goff, gsrc(6)); gld4(g[7], goff, gsrc(7)); }
#pragma unroll
    for (int n = 2; n < 8; ++n) {
      const int c0 = n < 5 ? 16 * n - 4 : 76 + 4 * (n - 5);
      if (F == c0 + 4) { settle(n); if (n == 2) STAMP(14); }
    }
#pragma unroll
    for (int n = 2; n < 8; ++n) {
      const int c0 = n < 5 ? 16 * n - 4 : 76 + 4 * (n - 5);
      if (F == c0) {
        if (n == 2) STAMP(13);
        const int yng = n < 5 ? 4 * (4 - n) + (G1AT <= c0 ? 12 : 0) : 4 * (7 - n);
        if (yng == 20) waitv<20>(); if (yng == 16) waitv<16>(); if (yng == 12) waitv<12>();
        if (yng == 8) waitv<8>(); if (yng == 4) waitv<4>(); if (yng == 0) waitv<0>();
        asm volatile("" : "+v"(g[n][0]), "+v"(g[n][1]), "+v"(g[n][2]), "+v"(g[n][3]));
        mx = 0;
      }
    }
#pragma unroll
    for (int s = 0; s < 2; ++s) {
      sbar();
      if (F + 7 < 128) {
        const int Fn = F + 7;
        rdaf(Fn, s);
        if (s == 1 && Fn >= 24 && Fn < 32) { va[Fn & 7] += BL - BH; asm volatile("" : "+v"(va[Fn & 7])); }
        if (s == 1 && Fn >= 72 && Fn < 80) { va[Fn & 7] += BH - BL; asm volatile("" : "+v"(va[Fn & 7])); }
      }
#pragma unroll
      for (int n = 2; n < 8; ++n) {
        const int c0 = n < 5 ? 16 * n - 4 : 76 + 4 * (n - 5);
        if (F >= c0 && F < c0 + 4) { mx = max3(mx, g[n][F - c0][2 * s], g[n][F - c0][2 * s + 1]); asm volatile("" : "+v"(mx)); }
      }
      if constexpr (F >= 93) {
        constexpr int J = (F - 93) / 12;
        const int e = ((F - 93) % 12) * 2 + s;
        if (e < 16) {
          const int s2 = e >> 3, e2 = e & 7;
          if (e2 < 4) { const f32x4 a = acc2[s2][2 * J + (e2 >> 1)]; od[s2][e2] = pk2(a[2 * (e2 & 1)], a[2 * (e2 & 1) + 1]); asm volatile("" : "+v"(od[s2][e2])); }
          if (e2 == 5 || e2 == 6) { auto r = __builtin_amdgcn_permlane16_swap(od[s2][e2 - 5], od[s2][e2 - 3], false, false); od[s2][e2 - 5] = r[0]; od[s2][e2 - 3] = r[1]; }
          if (e2 == 7) gsto<J * 64>(ooff[s2], od[s2], ro);
        }
      }
      sbar();
      if (F < 8) mfma_new(acc2[s][j], cat(af[F & 7][0], af[F & 7][1]), g[v][kk * 2 + s]);
      else mfma_acc(acc2[s][j], cat(af[F & 7][0], af[F & 7][1]), g[v][kk * 2 + s]);
    }
  });
  sbar();
  asm volatile("s_nop 7\n\ts_nop 3");
  sbar();
  STAMP(18);
#pragma unroll
  for (int s2 = 0; s2 < 2; ++s2) {
#pragma unroll
    for (int e2 = 0; e2 < 4; ++e2) { const f32x4 a = acc2[s2][6 + (e2 >> 1)]; od[s2][e2] = pk2(a[2 * (e2 & 1)], a[2 * (e2 & 1) + 1]); }
#pragma unroll
    for (int e2 = 0; e2 < 2; ++e2) { auto r = __builtin_amdgcn_permlane16_swap(od[s2][e2], od[s2][e2 + 2], false, false); od[s2][e2] = r[0]; od[s2][e2 + 2] = r[1]; }
    gsto<192>(ooff[s2], od[s2], ro);
  }
  STAMP(19);
  waitv<0>();
  STAMP(20);
  STAMP_OUT;
}

extern "C" void kernel_launch(void* const* d_in, const int* in_sizes, int n_in, void* d_out, int out_size, void* d_ws,
                              size_t ws_size, hipStream_t stream) {
  (void)hipFuncSetAttribute((const void*)k_conv_module, hipFuncAttributeMaxDynamicSharedMemorySize, LDSZ);
  k_conv_module<<<256, 512, LDSZ, stream>>>((const char*)d_in[0], (const char*)d_in[1], (const char*)d_in[2], (const char*)d_in[3],
                                            (char*)d_ws, (char*)d_out);
}
```

```cpp
#include <hip/hip_runtime.h>
#include <hip/hip_bf16.h>

typedef unsigned u32;
typedef unsigned long long u64;
typedef __attribute__((ext_vector_type(2))) unsigned u32x2;
typedef __attribute__((ext_vector_type(4))) unsigned u32x4;
typedef __attribute__((ext_vector_type(2))) unsigned long long u64x2;
typedef __attribute__((ext_vector_type(4))) unsigned long long u64x4;
typedef __attribute__((ext_vector_type(2))) __bf16 bf2;
typedef __attribute__((ext_vector_type(4))) __bf16 bf4;
typedef __attribute__((ext_vector_type(8))) __bf16 bf8;
typedef __attribute__((ext_vector_type(2))) float f32x2;
typedef __attribute__((ext_vector_type(4))) float f32x4;
#define LAS __attribute__((address_space(3)))
#define GAS __attribute__((address_space(1)))
#define DEVI __device__ __forceinline__
#define RLX_AGENT __ATOMIC_RELAXED, __HIP_MEMORY_SCOPE_AGENT

#define ROT 0
#define G0AT 12
#define G1AT 40

template <int N> DEVI void waitv() { asm volatile("s_waitcnt vmcnt(%0)" ::"n"(N) : "memory"); }
DEVI void sbar() { __builtin_amdgcn_sched_barrier(0); }
DEVI void waitl0() { sbar(); __builtin_amdgcn_s_waitcnt(0xC07F); sbar(); }
#define PINV(x) asm volatile("" : "+v"(x))
#define PINS(x) asm volatile("" : "+s"(x))
DEVI void bar() { asm volatile("s_barrier" ::: "memory"); }

DEVI u32 pk2(float a, float b) {
  f32x2 v = {a, b};
  bf2 r = __builtin_convertvector(v, bf2);
  return __builtin_bit_cast(u32, r);
}
DEVI f32x2 gate2(f32x2 a, f32x2 b) {
  const f32x2 t = b * -1.4426950408889634f;
  f32x2 e = {__builtin_amdgcn_exp2f(t[0]), __builtin_amdgcn_exp2f(t[1])};
  e = e + 1.0f;
  const f32x2 r = {__builtin_amdgcn_rcpf(e[0]), __builtin_amdgcn_rcpf(e[1])};
  return a * r;
}
#define LP(T, a) ((LAS T*)(size_t)(a))
DEVI bf8 ld128(u32 a) { return *LP(const bf8, a); }
DEVI bf4 ldtr(u32 a) { return __builtin_amdgcn_ds_read_tr16_b64_v4bf16(LP(bf4, a)); }
DEVI bf8 cat(bf4 a, bf4 b) { return __builtin_shufflevector(a, b, 0, 1, 2, 3, 4, 5, 6, 7); }
DEVI f32x4 mfma(bf8 a, bf8 b, f32x4 c) { return __builtin_amdgcn_mfma_f32_16x16x32_bf16(a, b, c, 0, 0, 0); }
#define NTX 0
#define NTW 0
#define NTV 0
template <int D, int S, int NT = 0> DEVI void glds(__amdgpu_buffer_rsrc_t r, u32 dst, u32 voff, u32 soff) {
  u32 t;
  if constexpr (NT)
    asm volatile("s_add_u32 m0, %1, %5\n\ts_add_u32 %0, %4, %6\n\tbuffer_load_dwordx4 %2, %3, %0 offen nt lds"
                 : "=&s"(t) : "s"(dst), "v"(voff), "s"(r), "s"(soff), "n"(D), "n"(S) : "memory", "scc");
  else
    asm volatile("s_add_u32 m0, %1, %5\n\ts_add_u32 %0, %4, %6\n\tbuffer_load_dwordx4 %2, %3, %0 offen lds"
                 : "=&s"(t) : "s"(dst), "v"(voff), "s"(r), "s"(soff), "n"(D), "n"(S) : "memory", "scc");
}
DEVI void gst(u32 voff, u32x4 d, char* base) {
  asm volatile("global_store_dwordx4 %0, %1, %2 sc1\n\ts_nop 1" ::"v"(voff), "v"(d), "s"(base) : "memory");
}
template <int O> DEVI void gsto(u32 voff, u32x4 d, __amdgpu_buffer_rsrc_t r) {
  asm volatile("buffer_store_dwordx4 %1, %0, %2, 0 offen offset:%3 sc1" ::"v"(voff), "v"(d), "s"(r), "n"(O) : "memory");
}
DEVI void mfma_acc(f32x4& c, bf8 a, u32x4 b) { asm volatile("v_mfma_f32_16x16x32_bf16 %0, %1, %2, %0" : "+v"(c) : "v"(a), "v"(b)); }
DEVI void mfma_new(f32x4& c, bf8 a, u32x4 b) { asm volatile("v_mfma_f32_16x16x32_bf16 %0, %1, %2, 0" : "=&v"(c) : "v"(a), "v"(b)); }
DEVI void gld4(u32x4* g, u32 voff, const char* base) {
  asm volatile("global_load_dwordx4 %0, %4, %5 sc1\n\tglobal_load_dwordx4 %1, %4, %5 offset:256 sc1\n\t"
               "global_load_dwordx4 %2, %4, %5 offset:2048 sc1\n\tglobal_load_dwordx4 %3, %4, %5 offset:2304 sc1"
               : "=&v"(g[0]), "=&v"(g[1]), "=&v"(g[2]), "=&v"(g[3])
               : "v"(voff), "s"(base) : "memory");
}
template <int V> struct IC { static constexpr int value = V; };
template <int I, int N, class Fn> DEVI void sfor(Fn&& f) {
  if constexpr (I < N) { f(IC<I>{}); sfor<I + 1, N>(f); }
}
DEVI u32 max3(u32 a, u32 b, u32 c) { return max(max(a, b), c); }

constexpr int SL = 51200;
constexpr int XO = 32768;
constexpr int DWOFF = 3 * SL;
constexpr int DUMMY = DWOFF + 4096;
constexpr int CTL = DWOFF + 5120;
constexpr int LDSZ = 163840;
constexpr int HSTR = 288;
constexpr int H0 = 2 * SL + XO, H1 = 0;
constexpr int BH = 69632, BL = 18432;
constexpr int VOFF[8] = {BH + 32768, BH + 49152, BL, BL + 16384, BL + 34816, BH, BH + 16384, BH + 32768};
constexpr int GSTR = 131072 + 256;

#define STAMP(i)
#define STAMP_INIT
#define STAMP_OUT
#define STAMP_PRE
#define STAMP_POST

constexpr int fV(int F) { return F < 80 ? F >> 4 : 5 + ((F - 80) % 12) / 4; }
constexpr int fK(int F) { return F < 80 ? (F >> 3) & 1 : (((F - 80) % 12) >> 1) & 1; }
constexpr int fJ(int F) { return F < 80 ? F & 7 : 2 * ((F - 80) / 12) + (F & 1); }

__global__ __launch_bounds__(512, 2) void k_conv_module(const char* x, const char* w1,
                                                        const char* dw, const char* w2,
                                                        char* ws, char* out) {
  STAMP_PRE;
  u64x4 ka;
  u64 kb;
  asm volatile("s_load_dwordx8 %0, %2, 0x0\n\ts_load_dwordx2 %1, %2, 0x20" : "=&s"(ka), "=&s"(kb) : "s"(__builtin_amdgcn_kernarg_segment_ptr()));
  sbar();
  const int tid = threadIdx.x, lane = tid & 63;
  const int w = __builtin_amdgcn_readfirstlane(tid >> 6);
  STAMP_INIT;
  STAMP(0);

  const int u = w & 3;
  const int b = blockIdx.x, xcd = b & 7, idx = b >> 3, gidx = idx >> 2, mem = idx & 3;
  const int grp = xcd * 8 + gidx, batch = grp >> 5, tile = grp & 31;
  const int t0 = tile * 125;
  const int k0 = ROT ? ((2 * mem + gidx) & 7) : 0;
  const int l15 = lane & 15, q = lane >> 4;
  if (w >= 4) {
    __builtin_amdgcn_s_setprio(3);
    u32 lb = u * 1024;
    u32 xo[5];
    xo[0] = (u32)((t0 - 8 + 8 * u + (lane >> 3)) * 1024 + (((lane & 7) ^ ((4 * u + q) & 7)) << 4));
#pragma unroll
    for (int i = 1; i < 5; ++i) xo[i] = xo[0] + i * 32768;
    if (u >= 2) xo[4] = 0x80000000u;
    const u32 swz = (u32)(mem * 256 + ((l15 ^ ((q << 2) | (u & 2))) << 4));
    u32 woff0 = (u32)((4 * u + q) * 2048) + swz;
    u32 voff0 = (u32)((4 * u + q) * 1024) + swz;
    u32 doff = (u32)((lane >> 2) * 1024 + mem * 256 + ((lane >> 1) & 1) * 128 + u * 32 + (lane & 1) * 16);
    u32 x5 = u < 2 ? lb + XO + 16384 : DUMMY, x5s = u < 2 ? SL : 0, xb = batch * 4096000;
    PINS(lb); PINS(x5); PINS(x5s); PINS(xb); PINV(woff0); PINV(voff0); PINV(doff);
#pragma unroll
    for (int i = 0; i < 5; ++i) PINV(xo[i]);
    sbar();
    asm volatile("s_waitcnt lgkmcnt(0)\n\ts_mov_b64 %0, %5\n\ts_mov_b64 %1, %6\n\ts_mov_b64 %2, %7\n\ts_mov_b64 %3, %8\n\ts_mov_b64 %4, %9"
                 : "=&s"(x), "=&s"(w1), "=&s"(dw), "=&s"(w2), "=&s"(ws) : "s"(ka[0]), "s"(ka[1]), "s"(ka[2]), "s"(ka[3]), "s"(kb));
    STAMP_POST;
    sbar();
    char* gbase = ws + (size_t)grp * GSTR;
    char* flags = gbase + 131072;
    const __amdgpu_buffer_rsrc_t rx = __builtin_amdgcn_make_buffer_rsrc((void*)(x + xb), 0, 4096000, 0x00020000);
    const __amdgpu_buffer_rsrc_t rw = __builtin_amdgcn_make_buffer_rsrc((void*)w1, 0, 1048576, 0x00020000);
    const __amdgpu_buffer_rsrc_t rd = __builtin_amdgcn_make_buffer_rsrc((void*)dw, 0, 15360, 0x00020000);
    const __amdgpu_buffer_rsrc_t rv = __builtin_amdgcn_make_buffer_rsrc((void*)w2, 0, 524288, 0x00020000);
    auto xunit = [&](auto Pc) {
      constexpr int P = Pc.value, d = XO + (P % 3) * SL, sc = ROT ? 0 : P * 128;
      const u32 so = ROT ? ((k0 + P) & 7) * 128 : 0;
      glds<d, sc, NTX>(rx, lb, xo[0], so);
      glds<d + 4096, sc, NTX>(rx, lb, xo[1], so);
      glds<d + 8192, sc, NTX>(rx, lb, xo[2], so);
      glds<d + 12288, sc, NTX>(rx, lb, xo[3], so);
      glds<0, sc, NTX>(rx, x5 + (P % 3) * x5s, xo[4], so);
    };
    auto wunit = [&](auto Pc, auto kc) {
      constexpr int P = Pc.value, ks = kc.value, d = (P % 3) * SL + ks * 16384, sc = (ROT ? 0 : P * 131072) + ks * 65536;
      const u32 so = ROT ? ((k0 + P) & 7) * 131072 : 0;
      glds<d, sc, NTW>(rw, lb, woff0, so);
      glds<d + 8192, sc + 1024, NTW>(rw, lb, woff0, so);
      glds<d + 4096, sc + 32768, NTW>(rw, lb, woff0, so);
      glds<d + 12288, sc + 32768 + 1024, NTW>(rw, lb, woff0, so);
    };
    auto punit = [&](auto Pc) { xunit(Pc); wunit(Pc, IC<0>{}); wunit(Pc, IC<1>{}); };
    auto vunit = [&](auto nc) {
      constexpr int n = nc.value;
      const u32 c = n < 2 ? 2 * mem : 2 * ((mem + 1 + (n - 2) % 3) & 3);
      constexpr int o = (n < 2 ? n : n >= 5) * 65536;
      glds<VOFF[n], o, NTV>(rv, lb, voff0, c * 65536);
      glds<VOFF[n] + 4096, o + 16384, NTV>(rv, lb, voff0, c * 65536);
      glds<VOFF[n] + 8192, o + 32768, NTV>(rv, lb, voff0, c * 65536);
      glds<VOFF[n] + 12288, o + 49152, NTV>(rv, lb, voff0, c * 65536);
    };
    char* gdst = gbase + (mem * 8 + u) * 1024;
    auto poison = [&](int blk) {
#pragma unroll
      for (int r = 0; r < 4; ++r) gst(lane * 16, u32x4{~0u, ~0u, ~0u, ~0u}, gdst + r * 32768 + blk * 4096);
    };
    u32x4 T[2][2][8];
    auto tbuild = [&](int blk, int hi) {
      u32x4 v[8];
#pragma unroll
      for (int j = 0; j < 8; ++j) {
        const int jj = j ^ (hi << 2);
        u32 i = (u32)(8 * q + jj - (l15 & 7) - 1);
        if ((l15 >> 3) != hi) i = 15;
        v[j] = *LP(const u32x4, DWOFF + lb + (min(i, 15u) << 6) + blk * 32 + hi * 16);
      }
#pragma unroll
      for (int c = 0; c < 8; ++c)
#pragma unroll
        for (int m = 0; m < 4; ++m)
          T[blk][hi][c][m] = __builtin_amdgcn_perm(v[2 * m + 1][c >> 1], v[2 * m][c >> 1], (c & 1) ? 0x07060302u : 0x05040100u);
#pragma unroll
      for (int c = 0; c < 8; ++c) asm volatile("" : "+v"(T[blk][hi][c]));
    };
    STAMP(21);
    punit(IC<0>{});
    STAMP(22);
    glds<DWOFF, 0>(rd, lb, doff, 0);
    punit(IC<1>{});
    __builtin_amdgcn_s_setprio(0);
    STAMP(20);
    waitv<18>(); bar();
    STAMP(1);
    xunit(IC<2>{}); waitv<19>(); bar();
    STAMP(14);
    wunit(IC<2>{}, IC<0>{}); wunit(IC<2>{}, IC<1>{}); waitv<17>(); bar();
    STAMP(15);
    xunit(IC<3>{}); waitv<18>(); bar();
    STAMP(16);
    wunit(IC<3>{}, IC<0>{}); wunit(IC<3>{}, IC<1>{}); waitv<13>(); bar();
    STAMP(2);
    u64 G;
    punit(IC<4>{}); poison(0);
    asm volatile("global_load_dwordx2 %0, %1, %2 offset:128 sc1" : "=&v"(G) : "v"(0), "s"(flags) : "memory");
    tbuild(0, 0); waitv<18>(); bar();
    STAMP(17);
    punit(IC<5>{}); poison(1); tbuild(0, 1); waitv<22>(); bar();
    STAMP(3);
    punit(IC<6>{}); tbuild(1, 0); waitv<17>(); bar();
    STAMP(18);
    PINV(G);
    punit(IC<7>{});
    u64 tag = (G ^ (G >> 29)) * 0x9E3779B97F4A7C15ull + 0x632BE59BD9B4E019ull;
    if (tag == G) ++tag;
    PINV(tag);
    tbuild(1, 1); waitv<13>(); bar();
    STAMP(4);
    if (u == 0 && lane == 0) asm volatile("global_store_dwordx2 %0, %1, %2 sc1" ::"v"(mem * 8), "v"(tag), "s"(flags) : "memory");
    vunit(IC<0>{}); vunit(IC<1>{}); waitv<8>(); bar();
    STAMP(19);
    vunit(IC<2>{}); vunit(IC<3>{}); bar();
    STAMP(5);
    vunit(IC<4>{}); vunit(IC<5>{}); vunit(IC<6>{});
    __builtin_amdgcn_s_setprio(1);
    u32 hr[2][2], hw[2];
    char* gd[2][4];
#pragma unroll
    for (int blk = 0; blk < 2; ++blk) {
      const u32 him = (blk ? H1 : H0) + u * (16 * HSTR);
      const int hoff = ((q == 3 ? 0 : q) + l15) * 16;
      hr[blk][0] = him + hoff; hr[blk][1] = him + (hoff ^ 16); hw[blk] = him + lane * 16;
      PINV(hr[blk][0]); PINV(hr[blk][1]); PINV(hw[blk]);
#pragma unroll
      for (int r = 0; r < 4; ++r) { gd[blk][r] = gdst + r * 32768 + blk * 4096; PINS(gd[blk][r]); }
    }
    u32 l16 = lane * 16;
    PINV(l16);
    waitv<20>();
#pragma unroll
    for (int blk = 0; blk < 2; ++blk) {
      sbar();
      bar();
      STAMP(6 + 3 * blk);
      bf8 wv[16];
#pragma unroll
      for (int c = 0; c < 8; ++c) {
        wv[c] = ld128(hr[blk][c >> 2] + c * HSTR);
        wv[c + 8] = ld128(hr[blk][c >> 2] + (c + 8) * HSTR);
      }
      sbar();
      f32x4 sv[8];
#pragma unroll
      for (int c = 0; c < 8; ++c) {
        f32x4 d = mfma(__builtin_bit_cast(bf8, T[blk][0][c]), wv[c], f32x4{0, 0, 0, 0});
        sv[c] = mfma(__builtin_bit_cast(bf8, T[blk][1][c]), wv[c + 8], d);
      }
#pragma unroll
      for (int c = 0; c < 8; ++c) {
        const f32x2 lo = gate2(sv[c].xy, sv[c].xy), hi = gate2(sv[c].zw, sv[c].zw);
        sv[c] = f32x4{lo[0], lo[1], hi[0], hi[1]};
      }
#pragma unroll
      for (int r = 0; r < 4; ++r) {
        const u32x4 d = u32x4{min(pk2(sv[0][r], sv[1][r]), ~1u), min(pk2(sv[2][r], sv[3][r]), ~1u),
                              min(pk2(sv[4][r], sv[5][r]), ~1u), min(pk2(sv[6][r], sv[7][r]), ~1u)};
        gst(l16, d, gd[blk][r]);
        *LP(u32x4, hw[blk] + r * 1024) = d;
      }
      STAMP(7 + 3 * blk);
      if (blk == 0) waitv<16>(); else waitv<8>();
      waitl0();
    }
    bar();
    STAMP(12);
    vunit(IC<7>{});
    waitv<0>();
    STAMP(13);
    bar();
    STAMP_OUT;
    return;
  }
  waitl0();
  STAMP_POST;
  const int qq = l15 >> 2, p = lane & 3;
  u32 trb = 256 * (8 * q + qq) + ((((p >> 1) ^ ((qq << 2) | ((q & 1) << 1)))) << 4) + 8 * (p & 1);
  u64 fl;
  char* gbase = ws + (size_t)grp * GSTR;
  char* flags = gbase + 131072;
  u32 va[8];
#pragma unroll
  for (int j = 0; j < 8; ++j) { va[j] = (trb ^ (j << 5)) + BH; PINV(va[j]); }
  u32 goff = (q >> 1) * 1024 + (q & 1) * 512 + l15 * 16;
  u32 gla = (q >> 1) * (16 * HSTR) + u * 1024 + (q & 1) * 512 + l15 * 16, gla0 = gla + H0;
  PINV(goff); PINV(gla); PINV(gla0);
  const char* gs[8];
#pragma unroll
  for (int n = 2; n < 8; ++n) { gs[n] = gbase + (u * 32 + ((mem + 1 + (n - 2) % 3) & 3) * 8 + (n >= 5) * 4) * 1024; PINS(gs[n]); }
  u32 ooff[2];
  ooff[0] = (l15 * 8 + u) * 1024 + (q & 1) * 32 + (q >> 1) * 16;
  ooff[1] = ooff[0] + 4096;
  PINV(ooff[0]); PINV(ooff[1]);
  __amdgpu_buffer_rsrc_t ro = __builtin_amdgcn_make_buffer_rsrc(out + ((size_t)batch * 4000 + t0) * 1024 + mem * 256, 0, 124 * 1024 + 256, 0x00020000);
  PINS(ro); PINS(flags);
  {
    u32 xa0 = XO + l15 * 128 + ((q ^ (l15 >> 1)) << 4), xa1 = xa0 ^ 64;
    u32 wa0 = trb ^ (u << 5), wa1 = wa0 ^ 128;
    f32x4 acc[9][4];
#pragma unroll
    for (int m = 0; m < 9; ++m)
#pragma unroll
      for (int n = 0; n < 4; ++n) { acc[m][n] = f32x4{0, 0, 0, 0}; PINV(acc[m][n]); }
    u32 hg = u * (16 * HSTR) + l15 * HSTR + ((((q >> 1) ^ ((l15 >> 2) & 1)) << 4) | (((q & 1) ^ (l15 >> 3)) << 3));
    u32 hg0 = hg + H0, fo = (lane & 3) * 8;
    PINV(hg); PINV(hg0); PINV(fo); PINV(xa0); PINV(xa1); PINV(wa0); PINV(wa1);
    bf8 xf[9];
    bf4 wf[2][4][2];
    __builtin_amdgcn_s_setprio(2);
    waitl0();
    bar();
    STAMP(1);
#pragma unroll
    for (int n = 0; n < 4; ++n) {
      wf[0][n][0] = ldtr((n >> 1 ? wa1 : wa0) + (n & 1) * 8192);
      wf[0][n][1] = ldtr((n >> 1 ? wa1 : wa0) + (n & 1) * 8192 + 1024);
      if (n == 0) xf[0] = ld128(xa0);
    }
#pragma unroll
    for (int m = 1; m < 8; ++m) xf[m] = ld128(xa0 + m * 2048);
    int sl = 0, dlt = 0;
#pragma unroll 1
    for (int P = 0; P < 8; ++P) {
#pragma unroll
      for (int ks = 0; ks < 2; ++ks) {
#pragma unroll
        for (int m = 0; m < 9; ++m) {
#pragma unroll
          for (int n = 0; n < 4; ++n) {
            sbar();
            const int s = m * 4 + n;
            if (s == 16) {
              if (ks == 0) { if (P < 2) { STAMP(P ? 4 : 2); bar(); } }
              else { waitl0(); STAMP(P == 0 ? 3 : P == 1 ? 8 : P == 2 ? 15 : P == 3 ? 21 : P == 4 ? 22 : 23); bar(); }
            }
            const bf8 xv = xf[m];
            const u32 xn = ks ? xa0 : xa1;
            const int wo = ks ? 0 : 16384;
            auto rw_ = [&](int n2, int t) { wf[ks ^ 1][n2][t] = ldtr((n2 >> 1 ? wa1 : wa0) + wo + (n2 & 1) * 8192 + t * 1024); };
            auto rx_ = [&](int m2) { xf[m2] = ld128(xn + m2 * 2048); };
            if (s == 1) xf[8] = ld128((ks ? xa1 : xa0) + 8 * 2048);
            if (ks == 1) {
              if (s == 2) { dlt = sl == 2 ? -2 * SL : SL; PINS(dlt); }
              if (s == 3) { sl = sl == 2 ? 0 : sl + 1; PINS(sl); }
              if (s == 5) { xa0 += dlt; asm volatile("" : "+v"(xa0)); }
              if (s == 6) { xa1 += dlt; asm volatile("" : "+v"(xa1)); }
              if (s == 9) { wa0 += dlt; asm volatile("" : "+v"(wa0)); }
              if (s == 10) { wa1 += dlt; asm volatile("" : "+v"(wa1)); }
            }
            if (s == 17) rw_(0, 0);
            if (s == 18) rw_(0, 1);
            if (s == 19) rx_(0);
            if (s == 20) rw_(1, 0);
            if (s == 21) rw_(1, 1);
            if (s == 22) rx_(1);
            if (s == 23) rw_(2, 0);
            if (s == 24) rw_(2, 1);
            if (s == 25) rx_(2);
            if (s == 26) rw_(3, 0);
            if (s == 27) rw_(3, 1);
            if (s == 28) rx_(3);
            if (s == 29) rx_(4);
            if (s == 30) rx_(5);
            if (s == 31) rx_(6);
            if (s == 33) rx_(7);
            sbar();
            acc[m][n] = mfma(xv, cat(wf[ks][n][0], wf[ks][n][1]), acc[m][n]);
          }
        }
      }
    }
    sbar();
    __builtin_amdgcn_s_setprio(0);
    STAMP(5);
    asm volatile("global_load_dwordx2 %0, %1, %2 sc1" : "=&v"(fl) : "v"(fo), "s"(flags) : "memory");
    sbar();
#pragma unroll
    for (int blk = 0; blk < 2; ++blk) {
      const u32 him = blk ? hg + H1 : hg0;
#pragma unroll
      for (int m = 0; m < 9; ++m) {
        const f32x4 a = acc[m][2 * blk], g = acc[m][2 * blk + 1];
        const f32x2 lo = gate2(a.xy, g.xy), hi = gate2(a.zw, g.zw);
        *LP(u32x2, him + m * 32) = u32x2{pk2(lo[0], lo[1]), pk2(hi[0], hi[1])};
      }
      STAMP(6 + 3 * blk);
      sbar();
      if (blk == 0) {
        waitl0();
        bar();
        sbar();
        STAMP(7);
      }
    }
  }
  waitv<0>();
  PINV(fl);
  auto alleq = [&]() {
    const u64 f0 = ((u64)__builtin_amdgcn_readfirstlane((u32)(fl >> 32)) << 32) | (u32)__builtin_amdgcn_readfirstlane((u32)fl);
    return __all(fl == f0);
  };
  if (__builtin_expect(!alleq(), 0)) {
#pragma unroll 1
    for (int it = 0; it < (1 << 20); ++it) {
      __builtin_amdgcn_s_sleep(1);
      asm volatile("global_load_dwordx2 %0, %1, %2 sc1\n\ts_waitcnt vmcnt(0)" : "=&v"(fl) : "v"((lane & 3) * 8), "s"(flags) : "memory");
      if (alleq()) break;
    }
  }
  if (w == 0 && mem == 0 && lane == 0) asm volatile("global_store_dwordx2 %0, %1, %2 offset:128 sc1" ::"v"(0), "v"(fl), "s"(flags) : "memory");
  f32x4 acc2[2][8];
  bf4 af[8][2];
  u32x4 g[8][4];
  auto gsrc = [&](int n) { return gs[n]; };
  auto rdaf = [&](int F, int t) {
    const int v = fV(F);
    af[F & 7][t] = ldtr(va[fJ(F)] + (VOFF[v] - (v >= 2 && v <= 4 ? BL : BH)) + fK(F) * 8192 + t * 1024);
  };
  u32 mx = 0;
  auto settle = [&](int n) {
    if (__builtin_expect(__any(mx == ~0u), 0)) {
#pragma unroll 1
      for (int it = 0; it < (1 << 18); ++it) {
        gld4(g[n], goff, gsrc(n));
        waitv<0>();
        asm volatile("" : "+v"(g[n][0]), "+v"(g[n][1]), "+v"(g[n][2]), "+v"(g[n][3]));
        u32 m2 = 0;
#pragma unroll
        for (int i = 0; i < 8; ++i) m2 = max3(m2, g[n][i >> 1][(i & 1) * 2], g[n][i >> 1][(i & 1) * 2 + 1]);
        if (!__any(m2 == ~0u)) break;
      }
    }
  };
#pragma unroll
  for (int F = 0; F < 7; ++F) { rdaf(F, 0); rdaf(F, 1); }
  u32x4 od[2];
  sbar();
  waitl0();
  bar();
  STAMP(10);
  sfor<0, 128>([&](auto Fc) {
    constexpr int F = Fc.value;
    constexpr int v = fV(F), j = fJ(F), kk = fK(F);
    sbar();
    if (F == 16) { STAMP(11); bar(); STAMP(12); }
    if (F == 80) { STAMP(16); bar(); STAMP(17); }
    if (F == 0 || F == 16) {
#pragma unroll
      for (int i = 0; i < 4; ++i) g[v][i] = __builtin_bit_cast(u32x4, ld128((v ? gla + H1 : gla0) + (i >> 1) * (32 * HSTR) + (i & 1) * 256));
    }
    if (F == G0AT) { gld4(g[2], goff, gsrc(2)); gld4(g[3], goff, gsrc(3)); gld4(g[4], goff, gsrc(4)); }
    if (F == G1AT) { gld4(g[5], goff, gsrc(5)); gld4(g[6], goff, gsrc(6)); gld4(g[7], goff, gsrc(7)); }
#pragma unroll
    for (int n = 2; n < 8; ++n) {
      const int c0 = n < 5 ? 16 * n - 4 : 76 + 4 * (n - 5);
      if (F == c0 + 4) { settle(n); if (n == 2) STAMP(14); }
    }
#pragma unroll
    for (int n = 2; n < 8; ++n) {
      const int c0 = n < 5 ? 16 * n - 4 : 76 + 4 * (n - 5);
      if (F == c0) {
        if (n == 2) STAMP(13);
        const int yng = n < 5 ? 4 * (4 - n) + (G1AT <= c0 ? 12 : 0) : 4 * (7 - n);
        if (yng == 20) waitv<20>(); if (yng == 16) waitv<16>(); if (yng == 12) waitv<12>();
        if (yng == 8) waitv<8>(); if (yng == 4) waitv<4>(); if (yng == 0) waitv<0>();
        asm volatile("" : "+v"(g[n][0]), "+v"(g[n][1]), "+v"(g[n][2]), "+v"(g[n][3]));
        mx = 0;
      }
    }
#pragma unroll
    for (int s = 0; s < 2; ++s) {
      sbar();
      if (F + 7 < 128) {
        const int Fn = F + 7;
        rdaf(Fn, s);
        if (s == 1 && Fn >= 24 && Fn < 32) { va[Fn & 7] += BL - BH; asm volatile("" : "+v"(va[Fn & 7])); }
        if (s == 1 && Fn >= 72 && Fn < 80) { va[Fn & 7] += BH - BL; asm volatile("" : "+v"(va[Fn & 7])); }
      }
#pragma unroll
      for (int n = 2; n < 8; ++n) {
        const int c0 = n < 5 ? 16 * n - 4 : 76 + 4 * (n - 5);
        if (F >= c0 && F < c0 + 4) { mx = max3(mx, g[n][F - c0][2 * s], g[n][F - c0][2 * s + 1]); asm volatile("" : "+v"(mx)); }
      }
      if constexpr (F >= 93) {
        constexpr int J = (F - 93) / 12;
        const int e = ((F - 93) % 12) * 2 + s;
        if (e < 16) {
          const int s2 = e >> 3, e2 = e & 7;
          if (e2 < 4) { const f32x4 a = acc2[s2][2 * J + (e2 >> 1)]; od[s2][e2] = pk2(a[2 * (e2 & 1)], a[2 * (e2 & 1) + 1]); asm volatile("" : "+v"(od[s2][e2])); }
          if (e2 == 5 || e2 == 6) { auto r = __builtin_amdgcn_permlane16_swap(od[s2][e2 - 5], od[s2][e2 - 3], false, false); od[s2][e2 - 5] = r[0]; od[s2][e2 - 3] = r[1]; }
          if (e2 == 7) gsto<J * 64>(ooff[s2], od[s2], ro);
        }
      }
      sbar();
      if (F < 8) mfma_new(acc2[s][j], cat(af[F & 7][0], af[F & 7][1]), g[v][kk * 2 + s]);
      else mfma_acc(acc2[s][j], cat(af[F & 7][0], af[F & 7][1]), g[v][kk * 2 + s]);
    }
  });
  sbar();
  asm volatile("s_nop 7\n\ts_nop 3");
  sbar();
  STAMP(18);
#pragma unroll
  for (int s2 = 0; s2 < 2; ++s2) {
#pragma unroll
    for (int e2 = 0; e2 < 4; ++e2) { const f32x4 a = acc2[s2][6 + (e2 >> 1)]; od[s2][e2] = pk2(a[2 * (e2 & 1)], a[2 * (e2 & 1) + 1]); }
#pragma unroll
    for (int e2 = 0; e2 < 2; ++e2) { auto r = __builtin_amdgcn_permlane16_swap(od[s2][e2], od[s2][e2 + 2], false, false); od[s2][e2] = r[0]; od[s2][e2 + 2] = r[1]; }
    gsto<192>(ooff[s2], od[s2], ro);
  }
  STAMP(19);
  STAMP(20);
  STAMP_OUT;
}

extern "C" void kernel_launch(void* const* d_in, const int* in_sizes, int n_in, void* d_out, int out_size, void* d_ws,
                              size_t ws_size, hipStream_t stream) {
  (void)hipFuncSetAttribute((const void*)k_conv_module, hipFuncAttributeMaxDynamicSharedMemorySize, LDSZ);
  k_conv_module<<<256, 512, LDSZ, stream>>>((const char*)d_in[0], (const char*)d_in[1], (const char*)d_in[2], (const char*)d_in[3],
                                            (char*)d_ws, (char*)d_out);
}
```

```cpp
#include <hip/hip_runtime.h>
#include <hip/hip_bf16.h>

typedef unsigned u32;
typedef unsigned long long u64;
typedef __attribute__((ext_vector_type(2))) unsigned u32x2;
typedef __attribute__((ext_vector_type(4))) unsigned u32x4;
typedef __attribute__((ext_vector_type(2))) unsigned long long u64x2;
typedef __attribute__((ext_vector_type(4))) unsigned long long u64x4;
typedef __attribute__((ext_vector_type(2))) __bf16 bf2;
typedef __attribute__((ext_vector_type(4))) __bf16 bf4;
typedef __attribute__((ext_vector_type(8))) __bf16 bf8;
typedef __attribute__((ext_vector_type(2))) float f32x2;
typedef __attribute__((ext_vector_type(4))) float f32x4;
typedef __attribute__((ext_vector_type(16))) float f32x16;
#define LAS __attribute__((address_space(3)))
#define GAS __attribute__((address_space(1)))
#define DEVI __device__ __forceinline__
#define RLX_AGENT __ATOMIC_RELAXED, __HIP_MEMORY_SCOPE_AGENT

#define PFC 1
#define DRY 0
#define PFAT 6
#define LAT 0, 6, 16, 34, 44, 54
#define LST 1, 2, 2, 2, 2, 2
#define TB0 4
#define PRE 0
#define SLP 0

template <int N> DEVI void waitv() { asm volatile("s_waitcnt vmcnt(%0)" ::"n"(N) : "memory"); }
DEVI void sbar() { __builtin_amdgcn_sched_barrier(0); }
DEVI void waitl0() { sbar(); __builtin_amdgcn_s_waitcnt(0xC07F); sbar(); }
#define PINV(x) asm volatile("" : "+v"(x))
#define PINS(x) asm volatile("" : "+s"(x))
DEVI void bar() { asm volatile("s_barrier" ::: "memory"); }

template <int N> DEVI void barlt(int p) {
  asm volatile("s_cmp_ge_u32 %0, %1\n\ts_cbranch_scc1 1f\n\ts_barrier\n1:" ::"s"(p), "n"(N) : "memory", "scc");
}

DEVI u32 pk2(float a, float b) {
  f32x2 v = {a, b};
  bf2 r = __builtin_convertvector(v, bf2);
  return __builtin_bit_cast(u32, r);
}
DEVI f32x2 gate2(f32x2 a, f32x2 b) {
  const f32x2 t = b * -1.4426950408889634f;
  f32x2 e = {__builtin_amdgcn_exp2f(t[0]), __builtin_amdgcn_exp2f(t[1])};
  e = e + 1.0f;
  const f32x2 r = {__builtin_amdgcn_rcpf(e[0]), __builtin_amdgcn_rcpf(e[1])};
  return a * r;
}
#define LP(T, a) ((LAS T*)(size_t)(a))
DEVI bf8 ld128(u32 a) { return *LP(const bf8, a); }
DEVI bf4 ldtr(u32 a) { return __builtin_amdgcn_ds_read_tr16_b64_v4bf16(LP(bf4, a)); }
DEVI bf8 cat(bf4 a, bf4 b) { return __builtin_shufflevector(a, b, 0, 1, 2, 3, 4, 5, 6, 7); }
DEVI f32x4 mfma(bf8 a, bf8 b, f32x4 c) { return __builtin_amdgcn_mfma_f32_16x16x32_bf16(a, b, c, 0, 0, 0); }
template <int D, int S> DEVI void glds(__amdgpu_buffer_rsrc_t r, u32 dst, u32 voff, u32 soff) {
  u32 t;
  asm volatile("s_add_u32 m0, %1, %5\n\ts_add_u32 %0, %4, %6\n\tbuffer_load_dwordx4 %2, %3, %0 offen lds"
               : "=&s"(t) : "s"(dst), "v"(voff), "s"(r), "s"(soff), "n"(D), "n"(S) : "memory", "scc");
}
DEVI void gst(u32 voff, u32x4 d, char* base) {
  asm volatile("global_store_dwordx4 %0, %1, %2 sc1\n\ts_nop 1" ::"v"(voff), "v"(d), "s"(base) : "memory");
}
template <int O> DEVI void gsto(u32 voff, u32x4 d, __amdgpu_buffer_rsrc_t r) {
  asm volatile("buffer_store_dwordx4 %1, %0, %2, 0 offen offset:%3 sc1" ::"v"(voff), "v"(d), "s"(r), "n"(O) : "memory");
}
DEVI void mfma_acc(f32x16& c, bf8 a, u32x4 b) { asm volatile("v_mfma_f32_32x32x16_bf16 %0, %1, %2, %0" : "+v"(c) : "v"(a), "v"(b)); }
DEVI void mfma_new(f32x16& c, bf8 a, u32x4 b) { asm volatile("v_mfma_f32_32x32x16_bf16 %0, %1, %2, 0" : "=&v"(c) : "v"(a), "v"(b)); }
template <int O> DEVI void gld(u32x4& g, u32 voff, const char* base) {
  asm volatile("global_load_dwordx4 %0, %1, %2 offset:%3 sc1" : "=&v"(g) : "v"(voff), "s"(base), "n"(O) : "memory");
}
DEVI void gld1(u32& g, u32 voff, const char* base) {
  asm volatile("global_load_dword %0, %1, %2 sc1" : "=&v"(g) : "v"(voff), "s"(base) : "memory");
}
template <int O> DEVI void grl(u32x4& g, u32 voff, const char* base) {
  asm volatile("global_load_dwordx4 %0, %1, %2 offset:%3 sc1" : "+v"(g) : "v"(voff), "s"(base), "n"(O) : "memory");
}
DEVI void grl1(u32& g, u32 voff, const char* base) {
  asm volatile("global_load_dword %0, %1, %2 sc1" : "+v"(g) : "v"(voff), "s"(base) : "memory");
}
DEVI u32 mix3(u32x4 d) { const u32 t = d[0] + d[1] + d[2]; return __builtin_amdgcn_alignbit(t, t, 13); }
#define DPPADD(d, s, c) asm volatile("v_add_u32_dpp %0, %1, %1 " c : "+v"(d) : "v"(s))
template <int V> struct IC { static constexpr int value = V; };
template <int I, int N, class Fn> DEVI void sfor(Fn&& f) {
  if constexpr (I < N) { f(IC<I>{}); sfor<I + 1, N>(f); }
}

constexpr int SL = 51200;
constexpr int XO = 32768;
constexpr int DWOFF = 3 * SL;
constexpr int DUMMY = DWOFF + 4096;
constexpr int CTL = DWOFF + 5120;
constexpr int LDSZ = 163840;
constexpr int HSTR = 288;
constexpr int H0 = 2 * SL + XO, H1 = 0;
constexpr int BH = 69632, BL = 18432;
constexpr int VOFF[8] = {BH + 32768, BH + 49152, BL, BL + 16384, BL + 34816, BH, BH + 16384, BH + 32768};
constexpr int CH = 4352;
constexpr int GSTR = 32 * CH + 256;

#define STAMP(i)
#define STAMP_INIT
#define STAMP_OUT
#define STAMP_PRE
#define STAMP_POST

constexpr int fV(int F) { return F < 80 ? F >> 4 : 5 + ((F - 80) % 12) / 4; }
constexpr int fK(int F) { return F < 80 ? (F >> 2) & 3 : F & 3; }
constexpr int fJ(int F) { return F < 80 ? F & 3 : (F - 80) / 12; }
constexpr int fC(int n) { return n < 5 ? 16 * n - 6 : 74 + 4 * (n - 5); }
constexpr int lat[6] = {LAT}, lst[6] = {LST};
constexpr int fL(int k) { return lat[k / 5] + (k % 5) * lst[k / 5]; }
constexpr bool fOk() { for (int k = 0; k < 30; ++k) if ((k && fL(k) < fL(k - 1)) || fL(k) > fC(2 + k / 5)) return false; return true; }
static_assert(fOk(), "");
constexpr int fY(int n) { int y = 0; for (int k = 5 * (n - 1); k < 30; ++k) y += fL(k) <= fC(n); return y; }

template <int n> DEVI void vunit(__amdgpu_buffer_rsrc_t rv, u32 lb, u32 voff, int mem) {
  const u32 c = n < 2 ? 2 * mem : 2 * ((mem + 1 + (n - 2) % 3) & 3);
  constexpr int o = (n < 2 ? n : n >= 5) * 65536;
  glds<VOFF[n], o>(rv, lb, voff, c * 65536);
  glds<VOFF[n] + 4096, o + 16384>(rv, lb, voff, c * 65536);
  glds<VOFF[n] + 8192, o + 32768>(rv, lb, voff, c * 65536);
  glds<VOFF[n] + 12288, o + 49152>(rv, lb, voff, c * 65536);
}

__global__ __launch_bounds__(512, 2) void k_conv_module(const char* x, const char* w1,
                                                        const char* dw, const char* w2,
                                                        char* ws, char* out) {
  STAMP_PRE;
  u64x4 ka;
  u64x2 kb;
  asm volatile("s_load_dwordx8 %0, %2, 0x0\n\ts_load_dwordx4 %1, %2, 0x20" : "=&s"(ka), "=&s"(kb) : "s"(__builtin_amdgcn_kernarg_segment_ptr()));
  if (PFC) {
    u64 pc0, ex;
    u32 po = ((blockIdx.x >> 3) * 5 + threadIdx.x) * 128;
    asm volatile("s_getpc_b64 %0\n\ts_mov_b32 m0, %3\n\tv_cmp_gt_u32 vcc, 5, %4\n\ts_and_saveexec_b64 %1, vcc\n\ts_cbranch_execz 1f\n\t"
                 "global_load_lds_dword %2, %0\n1:\n\ts_mov_b64 exec, %1"
                 : "=&s"(pc0), "=&s"(ex) : "v"(po), "n"(DUMMY), "v"(threadIdx.x) : "memory", "vcc", "scc");
  }
  sbar();
  const int tid = threadIdx.x, lane = tid & 63;
  const int w = __builtin_amdgcn_readfirstlane(tid >> 6);
  STAMP_INIT;
  STAMP(0);

  const int u = w & 3;
  const int b = blockIdx.x, xcd = b & 7, idx = b >> 3, gidx = idx >> 2, mem = idx & 3;
  const int grp = xcd * 8 + gidx, batch = grp >> 5, tile = grp & 31;
  const int t0 = tile * 125;
  const int l15 = lane & 15, q = lane >> 4;
  if (w < 4) {
  asm volatile("s_waitcnt lgkmcnt(0)\n\ts_mov_b64 %0, %3\n\ts_mov_b64 %1, %4\n\ts_mov_b64 %2, %5"
               : "=&s"(w2), "=&s"(ws), "=&s"(out) : "s"(ka[3]), "s"(kb[0]), "s"(kb[1]), "s"(ka));
  sbar();
  STAMP_POST;
  const int qq = l15 >> 2, p = lane & 3;
  u32 trb = 256 * (8 * q + qq) + ((((p >> 1) ^ ((qq << 2) | ((q & 1) << 1)))) << 4) + 8 * (p & 1);
  char* gbase = ws + (size_t)grp * GSTR;
  if (PFC) {
    const char* pa = lane == 0 ? gbase + (u * 8 + mem * 2) * CH : lane == 1 ? out + ((size_t)batch * 4000 + t0) * 1024 + mem * 256 : w2 + idx * 16384;
    u64 ex;
    asm volatile("s_mov_b32 m0, %2\n\tv_cmp_gt_u32 vcc, 3, %3\n\ts_and_saveexec_b64 %0, vcc\n\ts_cbranch_execz 1f\n\t"
                 "global_load_lds_dword %1, off\n1:\n\ts_mov_b64 exec, %0"
                 : "=&s"(ex) : "v"(pa), "n"(DUMMY), "v"(tid) : "memory", "vcc", "scc");
  }
  u32 va[4];
  const u32 trc = 256 * (8 * (q >> 1) + qq) + (((2 * (p & 1) + (q & 1)) ^ ((qq << 2) | (q & 2))) << 4) + 8 * (p >> 1);
#pragma unroll
  for (int j = 0; j < 4; ++j) { va[j] = (trc ^ (j << 6)) + BH; PINV(va[j]); }
  u32 goff = lane * 16, coff = 4096 + qq * 64 + q * 16 + p * 4;
  u32 gla = u * 1024 + lane * 16, gla0 = gla + H0;
  PINV(goff); PINV(gla); PINV(gla0); PINV(coff);
  const char* gs[8];
#pragma unroll
  for (int n = 2; n < 8; ++n) { gs[n] = gbase + (u * 8 + ((mem + 1 + (n - 2) % 3) & 3) * 2 + (n >= 5)) * CH; PINS(gs[n]); }
  u32 ooff[2];
  ooff[0] = (l15 * 8 + u) * 1024 + q * 16;
  ooff[1] = ooff[0] + 4096;
  PINV(ooff[0]); PINV(ooff[1]);
  __amdgpu_buffer_rsrc_t ro = __builtin_amdgcn_make_buffer_rsrc(out + ((size_t)batch * 4000 + t0) * 1024 + mem * 256, 0, 124 * 1024 + 256, 0x00020000);
  __amdgpu_buffer_rsrc_t rv = __builtin_amdgcn_make_buffer_rsrc((void*)w2, 0, 524288, 0x00020000);
  u32 voff0 = (u32)((4 * u + q) * 1024 + mem * 256 + ((l15 ^ ((q << 2) | (u & 2))) << 4)), lb = u * 1024;
  PINS(ro); PINS(gbase); PINS(rv); PINV(voff0); PINS(lb);
  {
    u32 xa0 = XO + l15 * 128 + ((q ^ (l15 >> 1)) << 4), xa1 = xa0 ^ 64;
    u32 wa0 = trb ^ (u << 5), wa1 = wa0 ^ 128;
    f32x4 acc[9][4];
    u32 hg = u * (16 * HSTR) + l15 * HSTR + ((((q >> 1) ^ ((l15 >> 2) & 1)) << 4) | (((q & 1) ^ (l15 >> 3)) << 3));
    u32 hg0 = hg + H0;
    if (DRY) { xa0 += 2 * SL; xa1 += 2 * SL; wa0 += 2 * SL; wa1 += 2 * SL; }
    PINV(hg); PINV(hg0); PINV(xa0); PINV(xa1); PINV(wa0); PINV(wa1);
    bf8 xf[9];
    bf4 wf[2][4][2];
    auto start = [&]() {
#pragma unroll
      for (int m = 0; m < 9; ++m)
#pragma unroll
        for (int n = 0; n < 4; ++n) { acc[m][n] = f32x4{0, 0, 0, 0}; PINV(acc[m][n]); }
      __builtin_amdgcn_s_setprio(2);
      waitl0();
      STAMP(2);
      bar();
      STAMP(1);
#pragma unroll
      for (int n = 0; n < 4; ++n) {
        wf[0][n][0] = ldtr((n >> 1 ? wa1 : wa0) + (n & 1) * 8192);
        wf[0][n][1] = ldtr((n >> 1 ? wa1 : wa0) + (n & 1) * 8192 + 1024);
        if (n == 0) xf[0] = ld128(xa0);
      }
#pragma unroll
      for (int m = 1; m < 8; ++m) xf[m] = ld128(xa0 + m * 2048);
    };
    if (DRY) {
#pragma unroll
      for (int m = 0; m < 9; ++m) {
        asm volatile("" : "=v"(xf[m]));
#pragma unroll
        for (int n = 0; n < 4; ++n) asm volatile("" : "=v"(acc[m][n]));
      }
#pragma unroll
      for (int n = 0; n < 4; ++n) asm volatile("" : "=v"(wf[0][n][0]), "=v"(wf[0][n][1]));
    } else start();
    int sl = DRY ? 2 : 0, dlt = 0;
#pragma unroll 1
    for (int P = -DRY; P < 8; ++P) {
#pragma unroll
      for (int ks = 0; ks < 2; ++ks) {
#pragma unroll
        for (int m = 0; m < 9; ++m) {
#pragma unroll
          for (int n = 0; n < 4; ++n) {
            sbar();
            const int s = m * 4 + n;
            if (s == 16) {
              if (ks == 0) barlt<2>(P);
              else { waitl0(); barlt<7>(P); }
            }
            const bf8 xv = xf[m];
            const u32 xn = ks ? xa0 : xa1;
            const int wo = ks ? 0 : 16384;
            auto rw_ = [&](int n2, int t) { wf[ks ^ 1][n2][t] = ldtr((n2 >> 1 ? wa1 : wa0) + wo + (n2 & 1) * 8192 + t * 1024); };
            auto rx_ = [&](int m2) { xf[m2] = ld128(xn + m2 * 2048); };
            if (s == 1) xf[8] = ld128((ks ? xa1 : xa0) + 8 * 2048);
            if (ks == 1) {
              if (s == 2) { dlt = sl == 2 ? -2 * SL : SL; PINS(dlt); }
              if (s == 3) { sl = sl == 2 ? 0 : sl + 1; PINS(sl); }
              if (s == 5) { xa0 += dlt; asm volatile("" : "+v"(xa0)); }
              if (s == 6) { xa1 += dlt; asm volatile("" : "+v"(xa1)); }
              if (s == 9) { wa0 += dlt; asm volatile("" : "+v"(wa0)); }
              if (s == 10) { wa1 += dlt; asm volatile("" : "+v"(wa1)); }
            }
            if (s == 17) rw_(0, 0);
            if (s == 18) rw_(0, 1);
            if (s == 19) rx_(0);
            if (s == 20) rw_(1, 0);
            if (s == 21) rw_(1, 1);
            if (s == 22) rx_(1);
            if (s == 23) rw_(2, 0);
            if (s == 24) rw_(2, 1);
            if (s == 25) rx_(2);
            if (s == 26) rw_(3, 0);
            if (s == 27) rw_(3, 1);
            if (s == 28) rx_(3);
            if (s == 29) rx_(4);
            if (s == 30) rx_(5);
            if (s == 31) rx_(6);
            if (s == 33) rx_(7);
            sbar();
            acc[m][n] = mfma(xv, cat(wf[ks][n][0], wf[ks][n][1]), acc[m][n]);
          }
        }
      }
      sbar();
      if (DRY && __builtin_expect(P < 0, 0)) start();
    }
    sbar();
    __builtin_amdgcn_s_setprio(0);
    STAMP(5);
#pragma unroll
    for (int blk = 0; blk < 2; ++blk) {
      const u32 him = blk ? hg + H1 : hg0;
#pragma unroll
      for (int m = 0; m < 9; ++m) {
        const f32x4 a = acc[m][2 * blk], g = acc[m][2 * blk + 1];
        const f32x2 lo = gate2(a.xy, g.xy), hi = gate2(a.zw, g.zw);
        *LP(u32x2, him + m * 32) = u32x2{pk2(lo[0], lo[1]), pk2(hi[0], hi[1])};
      }
      STAMP(6 + 3 * blk);
      sbar();
      if (blk == 0) {
        waitl0();
        bar();
        sbar();
        STAMP(7);
      }
    }
  }
  f32x16 acc2[4];
  bf4 af[8][2];
  u32x4 g[8][4];
  u32 ck[8];
  const u32 key = *LP(const u32, CTL), key4 = 4 * key;
  auto rdaf = [&](int F, int t) {
    const int v = fV(F);
    af[F & 7][t] = ldtr(va[fJ(F)] + (VOFF[v] - (v >= 2 && v <= 4 ? BL : BH)) + fK(F) * 4096 + t * 1024);
  };
  auto gload = [&](auto kc) {
    constexpr int k = kc.value, n = 2 + k / 5, i = k % 5;
    if constexpr (i < 4) gld<(i & 3) * 1024>(g[n][i & 3], goff, gs[n]); else gld1(ck[n], coff, gs[n]);
  };
  auto greload = [&](auto kc) {
    constexpr int k = kc.value, n = 2 + k / 5, i = k % 5;
    if constexpr (i < 4) grl<(i & 3) * 1024>(g[n][i & 3], goff, gs[n]); else grl1(ck[n], coff, gs[n]);
  };
  auto landed = [&](int n) { asm volatile("" : "+v"(g[n][0]), "+v"(g[n][1]), "+v"(g[n][2]), "+v"(g[n][3]), "+v"(ck[n])); };
  u64 bad;
  auto settle = [&](auto nc) {
    constexpr int n = nc.value, F = fC(n) + 6, m = n < 7 && fC(n + 1) < F ? n + 1 : n;
    if (__builtin_expect(bad != 0, 0)) {
#pragma unroll 1
      for (int it = 0; it < (1 << 18); ++it) {
        sfor<5 * (n - 2), 5 * (m - 1)>(greload);
        waitv<0>();
        landed(n); landed(m);
#pragma unroll
        for (int i = 0; i < 4; ++i) ck[n] += mix3(g[n][i]) + g[n][i][3];
        asm volatile("s_nop 1\n\tv_add_u32_dpp %0, %0, %0 row_ror:8 row_mask:0xf bank_mask:0xf\n\ts_nop 1\n\t"
                     "v_add_u32_dpp %0, %0, %0 row_ror:4 row_mask:0xf bank_mask:0xf" : "+v"(ck[n]));
        if (!__any(ck[n] != key4)) break;
      }
      if (m != n) {
#pragma unroll
        for (int i = 0; i < F - fC(m); ++i) ck[m] += mix3(g[m][i]) + g[m][i][3];
      }
      sfor<5 * (m - 1), 30>([&](auto kc) { if constexpr (fL(kc.value) < F) greload(kc); });
    }
  };
#pragma unroll
  for (int F = 0; F < 7; ++F) { rdaf(F, 0); rdaf(F, 1); }
  u32x4 od[2];
  sbar();
  waitl0();
  bar();
  STAMP(10);
  vunit<4>(rv, lb, voff0, mem); vunit<5>(rv, lb, voff0, mem); vunit<6>(rv, lb, voff0, mem);
  if (SLP) __builtin_amdgcn_s_sleep(SLP);
  sfor<0, 128>([&](auto Fc) {
    constexpr int F = Fc.value;
    constexpr int v = fV(F), j = fJ(F), kk = fK(F);
    sbar();
    if (F == 0) {
#pragma unroll
      for (int i = 0; i < 4; ++i) g[0][i] = __builtin_bit_cast(u32x4, ld128(gla0 + i * (16 * HSTR)));
    }
    if (F == PRE) { STAMP(11); bar(); STAMP(12); }
    if (F == 32) bar();
    if (F == 80) { STAMP(16); bar(); STAMP(17); }
    if (F == 44) STAMP(3);
    if (F == 60) STAMP(4);
    if (F == 72) STAMP(8);
    if (F == 90) STAMP(15);
    if (F == 101) STAMP(21);
    if (F == 104) STAMP(22);
    if (F == 113) STAMP(23);
    constexpr int G1F = PRE > 8 ? PRE : 8;
    if (F >= G1F && F < G1F + 4) g[1][(F - G1F) & 3] = __builtin_bit_cast(u32x4, ld128(gla + H1 + ((F - G1F) & 3) * (16 * HSTR)));
    sfor<2, 8>([&](auto nc) { if constexpr (F == fC(nc.value) + 6) { settle(nc); if (nc.value == 2) STAMP(14); } });
    sbar();
    sfor<0, 30>([&](auto kc) { if constexpr (fL(kc.value) == F) gload(kc); });
    u32 t;
    auto chk = [&](int ph) {
      sfor<2, 8>([&](auto nc) {
        constexpr int n = nc.value, e = F - fC(n);
        if (e == 0 && ph == 0) {
          if (n == 2) STAMP(13);
          waitv<fY(n)>();
          landed(n);
        }
        if (e >= 0 && e < 4) {
          const u32x4 d = g[n][e & 3];
          if (ph == 0) { t = d[0] + d[1] + d[2]; PINV(t); }
          if (ph == 1) { t = __builtin_amdgcn_alignbit(t, t, 13); PINV(t); }
          if (ph == 2) { ck[n] += t + d[3]; PINV(ck[n]); }
        }
        if (e == 4 && ph == 1) DPPADD(ck[n], ck[n], "row_ror:8 row_mask:0xf bank_mask:0xf");
        if (e == 5 && ph == 0) DPPADD(ck[n], ck[n], "row_ror:4 row_mask:0xf bank_mask:0xf");
        if (e == 5 && ph == 2) { bad = __ballot(ck[n] != key4); PINS(bad); }
        sbar();
      });
    };
    chk(0);
    if (F + 7 < 128) rdaf(F + 7, 0);
    sbar();
    chk(1);
    if (F + 7 < 128) {
      const int Fn = F + 7;
      rdaf(Fn, 1);
      sbar();
      if (Fn >= 28 && Fn < 32) { va[Fn & 3] += BL - BH; PINV(va[Fn & 3]); }
      if (Fn >= 76 && Fn < 80) { va[Fn & 3] += BH - BL; PINV(va[Fn & 3]); }
    }
    chk(2);
    if constexpr (F >= 95 && (F - 95) % 12 < 8) {
      constexpr int J = (F - 95) / 12, e = (F - 95) % 12;
#pragma unroll
      for (int i = 0; i < 2; ++i) {
        const int c = (2 * e + i) & 7;
        if (e < 4) { od[c >> 2][c & 3] = pk2(acc2[J][2 * c], acc2[J][2 * c + 1]); PINV(od[c >> 2][c & 3]); }
        else if (e < 6) { auto r = __builtin_amdgcn_permlane16_swap(od[0][c & 3], od[1][c & 3], false, false); od[0][c & 3] = r[0]; od[1][c & 3] = r[1]; }
      }
      if (e >= 6) gsto<J * 64>(ooff[e & 1], od[e & 1], ro);
    }
    sbar();
    if (F < 4) mfma_new(acc2[j], cat(af[F & 7][0], af[F & 7][1]), g[v][kk]);
    else mfma_acc(acc2[j], cat(af[F & 7][0], af[F & 7][1]), g[v][kk]);
  });
  sbar();
  asm volatile("s_nop 7\n\ts_nop 3");
  sbar();
  STAMP(18);
#pragma unroll
  for (int c = 0; c < 8; ++c) od[c >> 2][c & 3] = pk2(acc2[3][2 * c], acc2[3][2 * c + 1]);
#pragma unroll
  for (int c = 0; c < 4; ++c) { auto r = __builtin_amdgcn_permlane16_swap(od[0][c], od[1][c], false, false); od[0][c] = r[0]; od[1][c] = r[1]; }
  gsto<192>(ooff[0], od[0], ro);
  gsto<192>(ooff[1], od[1], ro);
  if (w == 0 && mem == 0 && lane == 0) asm volatile("global_store_dword %0, %1, %2 sc1" ::"v"(32 * CH), "v"(key), "s"(gbase) : "memory");
  STAMP(19);
  STAMP(20);
  STAMP_OUT;
  return;
  }
  {
    __builtin_amdgcn_s_setprio(3);
    u32 lb = u * 1024;
    u32 xo[5];
    xo[0] = (u32)((t0 - 8 + 8 * u + (lane >> 3)) * 1024 + (((lane & 7) ^ ((4 * u + q) & 7)) << 4));
#pragma unroll
    for (int i = 1; i < 5; ++i) xo[i] = xo[0] + i * 32768;
    if (u >= 2) xo[4] = 0x80000000u;
    const u32 swz = (u32)(mem * 256 + ((l15 ^ ((q << 2) | (u & 2))) << 4));
    u32 woff0 = (u32)((4 * u + q) * 2048) + swz;
    u32 voff0 = (u32)((4 * u + q) * 1024) + swz;
    u32 doff = (u32)((lane >> 2) * 1024 + mem * 256 + ((lane >> 1) & 1) * 128 + u * 32 + (lane & 1) * 16);
    u32 x5 = u < 2 ? lb + XO + 16384 : DUMMY, x5s = u < 2 ? SL : 0, xb = batch * 4096000;
    PINS(lb); PINS(x5); PINS(x5s); PINS(xb); PINV(woff0); PINV(voff0); PINV(doff);
#pragma unroll
    for (int i = 0; i < 5; ++i) PINV(xo[i]);
    sbar();
    asm volatile("s_waitcnt lgkmcnt(0)\n\ts_mov_b64 %0, %5\n\ts_mov_b64 %1, %6\n\ts_mov_b64 %2, %7\n\ts_mov_b64 %3, %8\n\ts_mov_b64 %4, %9"
                 : "=&s"(x), "=&s"(w1), "=&s"(dw), "=&s"(w2), "=&s"(ws) : "s"(ka[0]), "s"(ka[1]), "s"(ka[2]), "s"(ka[3]), "s"(kb[0]), "s"(kb));
    STAMP_POST;
    sbar();
    char* gbase = ws + (size_t)grp * GSTR;
    const __amdgpu_buffer_rsrc_t rx = __builtin_amdgcn_make_buffer_rsrc((void*)(x + xb), 0, 4096000, 0x00020000);
    const __amdgpu_buffer_rsrc_t rw = __builtin_amdgcn_make_buffer_rsrc((void*)w1, 0, 1048576, 0x00020000);
    const __amdgpu_buffer_rsrc_t rd = __builtin_amdgcn_make_buffer_rsrc((void*)dw, 0, 15360, 0x00020000);
    const __amdgpu_buffer_rsrc_t rv = __builtin_amdgcn_make_buffer_rsrc((void*)w2, 0, 524288, 0x00020000);
    auto pl = [&](auto Pc, auto ic) {
      constexpr int P = Pc.value, i = ic.value;
      if constexpr (i < 4) glds<XO + (P % 3) * SL + i * 4096, P * 128>(rx, lb, xo[i & 3], 0);
      else if constexpr (i == 4) glds<0, P * 128>(rx, x5 + (P % 3) * x5s, xo[4], 0);
      else {
        constexpr int ks = (i - 5) >> 2, hf = ((i - 5) >> 1) & 1, ab = (i - 5) & 1;
        glds<(P % 3) * SL + ks * 16384 + hf * 4096 + ab * 8192, P * 131072 + ks * 65536 + hf * 32768 + ab * 1024>(rw, lb, woff0, 0);
      }
    };
    auto xunit = [&](auto Pc) { sfor<0, 5>([&](auto ic) { pl(Pc, ic); }); };
    auto wunit = [&](auto Pc, auto kc) { sfor<5 + 4 * kc.value, 9 + 4 * kc.value>([&](auto ic) { pl(Pc, ic); }); };
    char* gdst = gbase + mem * 2 * CH;
    u32x4 T[2][2][8];
    auto punit = [&](auto Pc) {
      constexpr int tb = Pc.value - TB0, blk = (tb >> 1) & 1, hi = tb & 1;
      constexpr bool on = tb >= 0 && tb < 4;
      u32x4 v[8];
      sfor<0, 13>([&](auto ic) {
        constexpr int i = ic.value, c = (i - 5) & 7;
        pl(Pc, ic);
        sbar();
        if (on && i < 4) {
#pragma unroll
          for (int j = 2 * i; j < 2 * i + 2; ++j) {
            const int jj = j ^ (hi << 2);
            u32 r = (u32)(8 * q + jj - (l15 & 7) - 1);
            if ((l15 >> 3) != hi) r = 15;
            v[j & 7] = *LP(const u32x4, DWOFF + lb + (min(r, 15u) << 6) + blk * 32 + hi * 16);
          }
        }
        if (on && i >= 5) {
#pragma unroll
          for (int m = 0; m < 4; ++m)
            T[blk][hi][c][m] = __builtin_amdgcn_perm(v[2 * m + 1][c >> 1], v[2 * m][c >> 1], (c & 1) ? 0x07060302u : 0x05040100u);
          PINV(T[blk][hi][c]);
        }
        sbar();
      });
    };
    STAMP(21);
    punit(IC<0>{});
    STAMP(22);
    glds<DWOFF, 0>(rd, lb, doff, 0);
    xunit(IC<1>{});
    STAMP(20);
    waitv<10>(); bar();
    __builtin_amdgcn_s_setprio(0);
    STAMP(1);
    wunit(IC<1>{}, IC<0>{}); waitv<10>(); bar();
    STAMP(14);
    wunit(IC<1>{}, IC<1>{}); xunit(IC<2>{}); waitv<9>(); bar();
    STAMP(15);
    wunit(IC<2>{}, IC<0>{}); wunit(IC<2>{}, IC<1>{}); waitv<13>(); bar();
    STAMP(16);
    auto pfv = [&]() {
      const u32 po = lane < 32 ? (u32)((gidx * 64 + u * 16 + (lane >> 1)) * 1024 + mem * 256 + (lane & 1) * 128) : 0x80000000u;
      asm volatile("s_mov_b32 m0, %0\n\ts_nop 0\n\tbuffer_load_dword %1, %2, 0 offen lds" ::"s"(DUMMY), "v"(po), "s"(rv) : "memory");
    };
    punit(IC<3>{}); waitv<13>(); bar();
    STAMP(2);
    punit(IC<4>{}); if (PFAT == 4) pfv(); waitv<13 + (PFAT == 4)>(); bar();
    STAMP(17);
    punit(IC<5>{}); if (PFAT == 5) pfv(); waitv<13 + (PFAT == 4 || PFAT == 5)>(); bar();
    STAMP(3);
    punit(IC<6>{}); if (PFAT == 6) pfv(); waitv<13 + (PFAT == 5 || PFAT == 6)>(); bar();
    STAMP(18);
    punit(IC<7>{}); if (PFAT == 7) pfv(); waitv<13 + (PFAT == 6 || PFAT == 7)>(); bar();
    STAMP(4);
    u32 key;
    gld1(key, 32 * CH, gbase);
    vunit<0>(rv, lb, voff0, mem); vunit<1>(rv, lb, voff0, mem);
    waitv<9 + (PFAT == 7)>(); bar();
    STAMP(19);
    vunit<2>(rv, lb, voff0, mem); vunit<3>(rv, lb, voff0, mem);
    STAMP(5);
    __builtin_amdgcn_s_setprio(1);
    u32 hr[2][2], hw[2];
    char* gd[2][4];
#pragma unroll
    for (int blk = 0; blk < 2; ++blk) {
      const u32 him = (blk ? H1 : H0) + u * (16 * HSTR);
      const int hoff = ((q == 3 ? 0 : q) + l15) * 16;
      hr[blk][0] = him + hoff; hr[blk][1] = him + (hoff ^ 16); hw[blk] = him + lane * 16;
      PINV(hr[blk][0]); PINV(hr[blk][1]); PINV(hw[blk]);
#pragma unroll
      for (int r = 0; r < 4; ++r) { gd[blk][r] = gdst + (r * 8 + blk) * CH; PINS(gd[blk][r]); }
    }
    u32 l16 = u * 1024 + lane * 16, co = (l15 >> 2) * (8 * CH) + 4096 + u * 64 + q * 16 + (lane & 3) * 4;
    PINV(l16); PINV(co);
    waitv<12>();
    PINV(key);
    ++key;
    if (lane == 0) *LP(u32, CTL) = key;
    waitl0();
#pragma unroll
    for (int blk = 0; blk < 2; ++blk) {
      sbar();
      bar();
      STAMP(6 + 3 * blk);
      bf8 wv[16];
#pragma unroll
      for (int c = 0; c < 8; ++c) {
        wv[c] = ld128(hr[blk][c >> 2] + c * HSTR);
        wv[c + 8] = ld128(hr[blk][c >> 2] + (c + 8) * HSTR);
      }
      sbar();
      f32x4 sv[8];
#pragma unroll
      for (int c = 0; c < 8; ++c) {
        f32x4 d = mfma(__builtin_bit_cast(bf8, T[blk][0][c]), wv[c], f32x4{0, 0, 0, 0});
        sv[c] = mfma(__builtin_bit_cast(bf8, T[blk][1][c]), wv[c + 8], d);
      }
#pragma unroll
      for (int c = 0; c < 8; ++c) {
        const f32x2 lo = gate2(sv[c].xy, sv[c].xy), hi = gate2(sv[c].zw, sv[c].zw);
        sv[c] = f32x4{lo[0], lo[1], hi[0], hi[1]};
      }
      u32 cs[4];
#pragma unroll
      for (int r = 0; r < 4; ++r) {
        const u32x4 d = u32x4{pk2(sv[0][r], sv[1][r]), pk2(sv[2][r], sv[3][r]), pk2(sv[4][r], sv[5][r]), pk2(sv[6][r], sv[7][r])};
        gst(l16, d, gd[blk][r]);
        *LP(u32x4, hw[blk] + r * 1024) = d;
        cs[r] = mix3(d) + d[3];
      }
      asm volatile("s_nop 1\n\t"
                   "v_add_u32_dpp %0, %0, %0 row_ror:8 row_mask:0xf bank_mask:0x3\n\t"
                   "v_add_u32_dpp %1, %1, %1 row_ror:8 row_mask:0xf bank_mask:0x3\n\t"
                   "v_add_u32_dpp %0, %2, %2 row_ror:8 row_mask:0xf bank_mask:0xc\n\t"
                   "v_add_u32_dpp %1, %3, %3 row_ror:8 row_mask:0xf bank_mask:0xc\n\t"
                   "s_nop 0\n\t"
                   "v_add_u32_dpp %0, %0, %0 row_shl:4 row_mask:0xf bank_mask:0x5\n\t"
                   "v_add_u32_dpp %0, %1, %1 row_shr:4 row_mask:0xf bank_mask:0xa\n\t"
                   "v_sub_u32 %0, %4, %0\n\t"
                   "global_store_dword %5, %0, %6 sc1"
                   : "+v"(cs[0]), "+v"(cs[1]) : "v"(cs[2]), "v"(cs[3]), "v"(key), "v"(co), "s"(gd[blk][0]) : "memory");
      STAMP(7 + 3 * blk);
      if (blk == 0) waitv<13>(); else waitv<10>();
      waitl0();
    }
    bar();
    STAMP(12);
    bar();
    vunit<7>(rv, lb, voff0, mem);
    waitv<0>();
    STAMP(13);
    bar();
    STAMP_OUT;
  }
}

extern "C" void kernel_launch(void* const* d_in, const int* in_sizes, int n_in, void* d_out, int out_size, void* d_ws,
                              size_t ws_size, hipStream_t stream) {
  (void)hipFuncSetAttribute((const void*)k_conv_module, hipFuncAttributeMaxDynamicSharedMemorySize, LDSZ);
  k_conv_module<<<256, 512, LDSZ, stream>>>((const char*)d_in[0], (const char*)d_in[1], (const char*)d_in[2], (const char*)d_in[3],
                                            (char*)d_ws, (char*)d_out);
}
```

```cpp
#include <hip/hip_runtime.h>
#include <hip/hip_bf16.h>

typedef unsigned u32;
typedef unsigned long long u64;
typedef __attribute__((ext_vector_type(2))) unsigned u32x2;
typedef __attribute__((ext_vector_type(4))) unsigned u32x4;
typedef __attribute__((ext_vector_type(2))) unsigned long long u64x2;
typedef __attribute__((ext_vector_type(4))) unsigned long long u64x4;
typedef __attribute__((ext_vector_type(2))) __bf16 bf2;
typedef __attribute__((ext_vector_type(4))) __bf16 bf4;
typedef __attribute__((ext_vector_type(8))) __bf16 bf8;
typedef __attribute__((ext_vector_type(2))) float f32x2;
typedef __attribute__((ext_vector_type(4))) float f32x4;
typedef __attribute__((ext_vector_type(16))) float f32x16;
#define LAS __attribute__((address_space(3)))
#define GAS __attribute__((address_space(1)))
#define DEVI __device__ __forceinline__
#define RLX_AGENT __ATOMIC_RELAXED, __HIP_MEMORY_SCOPE_AGENT

#define PFC 1
#define PFAT 6
#define LAT 10, 22, 36, 46, 52, 58
#define LST 1, 2, 2, 1, 1, 1
#define TB0 4
#define HV 0
#define CV 1
#define NM 4
#define G0 6
#define GPF 0
#define PRE 0
#define SLP 0

template <int N> DEVI void waitv() { asm volatile("s_waitcnt vmcnt(%0)" ::"n"(N) : "memory"); }
DEVI void sbar() { __builtin_amdgcn_sched_barrier(0); }
DEVI void waitl0() { sbar(); __builtin_amdgcn_s_waitcnt(0xC07F); sbar(); }
#define PINV(x) asm volatile("" : "+v"(x))
#define PINS(x) asm volatile("" : "+s"(x))
DEVI void bar() { asm volatile("s_barrier" ::: "memory"); }

template <int N> DEVI void barlt(int p) {
  asm volatile("s_cmp_ge_u32 %0, %1\n\ts_cbranch_scc1 1f\n\ts_barrier\n1:" ::"s"(p), "n"(N) : "memory", "scc");
}

DEVI u32 pk2(float a, float b) {
  f32x2 v = {a, b};
  bf2 r = __builtin_convertvector(v, bf2);
  return __builtin_bit_cast(u32, r);
}
DEVI f32x2 gate2(f32x2 a, f32x2 b) {
  const f32x2 t = b * -1.4426950408889634f;
  f32x2 e = {__builtin_amdgcn_exp2f(t[0]), __builtin_amdgcn_exp2f(t[1])};
  e = __builtin_elementwise_fma(e, f32x2{-0.6931471805599453f, -0.6931471805599453f}, f32x2{-0.6931471805599453f, -0.6931471805599453f});
  const f32x2 r = {__builtin_amdgcn_rcpf(e[0]), __builtin_amdgcn_rcpf(e[1])};
  return a * r;
}
DEVI f32x2 silu2(f32x2 z) {
  f32x2 e = {__builtin_amdgcn_exp2f(z[0]), __builtin_amdgcn_exp2f(z[1])};
  e = __builtin_elementwise_fma(e, f32x2{-1.4426950408889634f, -1.4426950408889634f}, f32x2{-1.4426950408889634f, -1.4426950408889634f});
  const f32x2 r = {__builtin_amdgcn_rcpf(e[0]), __builtin_amdgcn_rcpf(e[1])};
  return z * r;
}
#define LP(T, a) ((LAS T*)(size_t)(a))
DEVI bf8 ld128(u32 a) { return *LP(const bf8, a); }
DEVI bf4 ldtr(u32 a) { return __builtin_amdgcn_ds_read_tr16_b64_v4bf16(LP(bf4, a)); }
DEVI bf8 cat(bf4 a, bf4 b) { return __builtin_shufflevector(a, b, 0, 1, 2, 3, 4, 5, 6, 7); }
DEVI f32x4 mfma(bf8 a, bf8 b, f32x4 c) { return __builtin_amdgcn_mfma_f32_16x16x32_bf16(a, b, c, 0, 0, 0); }
template <int D, int S> DEVI void glds(__amdgpu_buffer_rsrc_t r, u32 dst, u32 voff, u32 soff) {
  u32 t;
  asm volatile("s_add_u32 m0, %1, %5\n\ts_add_u32 %0, %4, %6\n\tbuffer_load_dwordx4 %2, %3, %0 offen lds"
               : "=&s"(t) : "s"(dst), "v"(voff), "s"(r), "s"(soff), "n"(D), "n"(S) : "memory", "scc");
}
DEVI void gst(u32 voff, u32x4 d, char* base) {
  asm volatile("global_store_dwordx4 %0, %1, %2 sc1\n\ts_nop 1" ::"v"(voff), "v"(d), "s"(base) : "memory");
}
template <int O> DEVI void gsto(u32 voff, u32x4 d, __amdgpu_buffer_rsrc_t r) {
  asm volatile("buffer_store_dwordx4 %1, %0, %2, 0 offen offset:%3 sc1" ::"v"(voff), "v"(d), "s"(r), "n"(O) : "memory");
}
DEVI void mfma16(f32x4& c, bf8 a, bf8 b) { asm volatile("v_mfma_f32_16x16x32_bf16 %0, %1, %2, %0" : "+v"(c) : "v"(a), "v"(b)); }
DEVI void mfma16n(f32x4& c, bf8 a, bf8 b) { asm volatile("v_mfma_f32_16x16x32_bf16 %0, %1, %2, 0" : "=&v"(c) : "v"(a), "v"(b)); }
DEVI void mfma_acc(f32x16& c, bf8 a, u32x4 b) { asm volatile("v_mfma_f32_32x32x16_bf16 %0, %1, %2, %0" : "+v"(c) : "v"(a), "v"(b)); }
DEVI void mfma_new(f32x16& c, bf8 a, u32x4 b) { asm volatile("v_mfma_f32_32x32x16_bf16 %0, %1, %2, 0" : "=&v"(c) : "v"(a), "v"(b)); }
template <int O> DEVI void gld(u32x4& g, u32 voff, const char* base) {
  asm volatile("global_load_dwordx4 %0, %1, %2 offset:%3 sc1" : "=&v"(g) : "v"(voff), "s"(base), "n"(O) : "memory");
}
DEVI void gld1(u32& g, u32 voff, const char* base) {
  asm volatile("global_load_dword %0, %1, %2 sc1" : "=&v"(g) : "v"(voff), "s"(base) : "memory");
}
template <int O> DEVI void grl(u32x4& g, u32 voff, const char* base) {
  asm volatile("global_load_dwordx4 %0, %1, %2 offset:%3 sc1" : "+v"(g) : "v"(voff), "s"(base), "n"(O) : "memory");
}
DEVI void grl1(u32& g, u32 voff, const char* base) {
  asm volatile("global_load_dword %0, %1, %2 sc1" : "+v"(g) : "v"(voff), "s"(base) : "memory");
}
DEVI u32 mix3(u32x4 d) { const u32 t = d[0] + d[1] + d[2]; return __builtin_amdgcn_alignbit(t, t, 13); }
#define DPPADD(d, s, c) asm volatile("v_add_u32_dpp %0, %1, %1 " c : "+v"(d) : "v"(s))
template <int V> struct IC { static constexpr int value = V; };
template <int I, int N, class Fn> DEVI void sfor(Fn&& f) {
  if constexpr (I < N) { f(IC<I>{}); sfor<I + 1, N>(f); }
}

constexpr int SL = 51200;
constexpr int XO = 32768;
constexpr int DWOFF = 3 * SL;
constexpr int DUMMY = DWOFF + 4096;
constexpr int CTL = DWOFF + 5120;
constexpr int LDSZ = 163840;
constexpr int HSTR = 288;
constexpr int H0 = 2 * SL + XO, H1 = 0;
constexpr int BH = 69632, BL = 18432;
constexpr int VOFF[8] = {BH + 32768, BH + 49152, BL, BL + 16384, BL + 34816, BH, BH + 16384, BH + 32768};
constexpr int CH = 4352;
constexpr int GSTR = 32 * CH + 256;

#define STAMP(i)
#define STAMP_INIT
#define STAMP_OUT
#define STAMP_PRE
#define STAMP_POST

constexpr int fV(int F) { return F < 80 ? F >> 4 : 5 + ((F - 80) % 12) / 4; }
constexpr int fK(int F) { return F < 80 ? (F >> 2) & 3 : F & 3; }
constexpr int fJ(int F) { return F < 80 ? F & 3 : (F - 80) / 12; }
constexpr int fC(int n) { return n < 5 ? 16 * n - 6 : 74 + 4 * (n - 5); }
constexpr int lat[6] = {LAT}, lst[6] = {LST};
constexpr int fL(int k) { return lat[k / 5] + (k % 5) * lst[k / 5]; }
constexpr bool fOk() { for (int k = 0; k < 30; ++k) if ((k && fL(k) < fL(k - 1)) || fL(k) > fC(2 + k / 5)) return false; return true; }
static_assert(fOk(), "");
constexpr int fY(int n) { int y = 0; for (int k = 5 * (n - 1); k < 30; ++k) y += fL(k) <= fC(n); return y; }

template <int n> DEVI void vunit(__amdgpu_buffer_rsrc_t rv, u32 lb, u32 voff, int mem) {
  const u32 c = n < 2 ? 2 * mem : 2 * ((mem + 1 + (n - 2) % 3) & 3);
  constexpr int o = (n < 2 ? n : n >= 5) * 65536;
  glds<VOFF[n], o>(rv, lb, voff, c * 65536);
  glds<VOFF[n] + 4096, o + 16384>(rv, lb, voff, c * 65536);
  glds<VOFF[n] + 8192, o + 32768>(rv, lb, voff, c * 65536);
  glds<VOFF[n] + 12288, o + 49152>(rv, lb, voff, c * 65536);
}

__global__ __launch_bounds__(512, 2) void k_conv_module(const char* x, const char* w1,
                                                        const char* dw, const char* w2,
                                                        char* ws, char* out) {
  STAMP_PRE;
  u64x4 ka;
  u64x2 kb;
  asm volatile("s_load_dwordx8 %0, %2, 0x0\n\ts_load_dwordx4 %1, %2, 0x20" : "=&s"(ka), "=&s"(kb) : "s"(__builtin_amdgcn_kernarg_segment_ptr()));
  if (PFC) {
    u64 pc0, ex;
    u32 po = ((blockIdx.x >> 3) * 5 + threadIdx.x) * 128;
    asm volatile("s_getpc_b64 %0\n\ts_mov_b32 m0, %3\n\tv_cmp_gt_u32 vcc, 5, %4\n\ts_and_saveexec_b64 %1, vcc\n\ts_cbranch_execz 1f\n\t"
                 "global_load_lds_dword %2, %0\n1:\n\ts_mov_b64 exec, %1"
                 : "=&s"(pc0), "=&s"(ex) : "v"(po), "n"(DUMMY), "v"(threadIdx.x) : "memory", "vcc", "scc");
  }
  sbar();
  const int tid = threadIdx.x, lane = tid & 63;
  const int w = __builtin_amdgcn_readfirstlane(tid >> 6);
  STAMP_INIT;
  STAMP(0);

  const int u = w & 3;
  const int b = blockIdx.x, xcd = b & 7, idx = b >> 3, gidx = idx >> 2, mem = idx & 3;
  const int grp = xcd * 8 + gidx, batch = grp >> 5, tile = grp & 31;
  const int t0 = tile * 125;
  const int l15 = lane & 15, q = lane >> 4;
  if (w < 4) {
  asm volatile("s_waitcnt lgkmcnt(0)\n\ts_mov_b64 %0, %3\n\ts_mov_b64 %1, %4\n\ts_mov_b64 %2, %5"
               : "=&s"(w2), "=&s"(ws), "=&s"(out) : "s"(ka[3]), "s"(kb[0]), "s"(kb[1]), "s"(ka));
  sbar();
  STAMP_POST;
  const int qq = l15 >> 2, p = lane & 3;
  u32 trb = 256 * (8 * q + qq) + ((((p >> 1) ^ ((qq << 2) | ((q & 1) << 1)))) << 4) + 8 * (p & 1);
  char* gbase = ws + (size_t)grp * GSTR;
  if (PFC) {
    const char* pa = lane == 0 ? gbase + (u * 8 + mem * 2) * CH : lane == 1 ? out + ((size_t)batch * 4000 + t0) * 1024 + mem * 256 : w2 + idx * 16384;
    u64 ex;
    asm volatile("s_mov_b32 m0, %2\n\tv_cmp_gt_u32 vcc, 3, %3\n\ts_and_saveexec_b64 %0, vcc\n\ts_cbranch_execz 1f\n\t"
                 "global_load_lds_dword %1, off\n1:\n\ts_mov_b64 exec, %0"
                 : "=&s"(ex) : "v"(pa), "n"(DUMMY), "v"(tid) : "memory", "vcc", "scc");
  }
  u32 va[4];
  const u32 trc = 256 * (8 * (q >> 1) + qq) + (((2 * (p & 1) + (q & 1)) ^ ((qq << 2) | (q & 2))) << 4) + 8 * (p >> 1);
#pragma unroll
  for (int j = 0; j < 4; ++j) { va[j] = (trc ^ (j << 6)) + BH; PINV(va[j]); }
  u32 goff = lane * 16, coff = 4096 + qq * 64 + q * 16 + p * 4;
  u32 gla = u * 1024 + lane * 16, gla0 = gla + H0;
  PINV(goff); PINV(gla); PINV(gla0); PINV(coff);
  const char* gs[8];
#pragma unroll
  for (int n = 2; n < 8; ++n) { gs[n] = gbase + (u * 8 + ((mem + 1 + (n - 2) % 3) & 3) * 2 + (n >= 5)) * CH; PINS(gs[n]); }
  u32 ooff[2];
  ooff[0] = (l15 * 8 + u) * 1024 + q * 16;
  ooff[1] = ooff[0] + 4096;
  PINV(ooff[0]); PINV(ooff[1]);
  __amdgpu_buffer_rsrc_t ro = __builtin_amdgcn_make_buffer_rsrc(out + ((size_t)batch * 4000 + t0) * 1024 + mem * 256, 0, 124 * 1024 + 256, 0x00020000);
  __amdgpu_buffer_rsrc_t rv = __builtin_amdgcn_make_buffer_rsrc((void*)w2, 0, 524288, 0x00020000);
  u32 voff0 = (u32)((4 * u + q) * 1024 + mem * 256 + ((l15 ^ ((q << 2) | (u & 2))) << 4)), lb = u * 1024;
  PINS(ro); PINS(gbase); PINS(rv); PINV(voff0); PINS(lb);
  {
    u32 xa0 = XO + l15 * 128 + ((q ^ (l15 >> 1)) << 4), xa1 = xa0 ^ 64;
    u32 wa0 = trb ^ (u << 5), wa1 = wa0 ^ 128;
    f32x4 acc[9][4];
    u32 hg = u * (16 * HSTR) + l15 * HSTR + ((((q >> 1) ^ ((l15 >> 2) & 1)) << 4) | (((q & 1) ^ (l15 >> 3)) << 3));
    u32 hg0 = hg + H0;
    PINV(hg); PINV(hg0); PINV(xa0); PINV(xa1); PINV(wa0); PINV(wa1);
    bf8 xf[9];
    bf4 wf[2][4][2];
    auto start = [&]() {
      __builtin_amdgcn_s_setprio(2);
      waitl0();
      STAMP(2);
      bar();
      STAMP(1);
#pragma unroll
      for (int n = 0; n < 4; ++n) {
        wf[0][n][0] = ldtr((n >> 1 ? wa1 : wa0) + (n & 1) * 8192);
        wf[0][n][1] = ldtr((n >> 1 ? wa1 : wa0) + (n & 1) * 8192 + 1024);
        if (n == 0) xf[0] = ld128(xa0);
      }
#pragma unroll
      for (int m = 1; m < 8; ++m) xf[m] = ld128(xa0 + m * 2048);
    };
    start();
    int sl = 0, dlt = 0;
    auto kstep = [&](auto ksc, int P) {
      {
        constexpr int ks = ksc.value & 1;
#pragma unroll
        for (int m = 0; m < 9; ++m) {
#pragma unroll
          for (int n = 0; n < 4; ++n) {
            sbar();
            const int s = m * 4 + n;
            if (s == 16) {
              if (ksc.value == 2) bar();
              else if (ks == 0) barlt<2>(P);
              else { waitl0(); bar(); }
            }
            const bf8 xv = xf[m];
            const u32 xn = ks ? xa0 : xa1;
            const int wo = ks ? 0 : 16384;
            auto rw_ = [&](int n2, int t) { wf[ks ^ 1][n2][t] = ldtr((n2 >> 1 ? wa1 : wa0) + wo + (n2 & 1) * 8192 + t * 1024); };
            auto rx_ = [&](int m2) { xf[m2] = ld128(xn + m2 * 2048); };
            if (s == 1) xf[8] = ld128((ks ? xa1 : xa0) + 8 * 2048);
            if (ks == 1) {
              if (s == 2) { dlt = sl == 2 ? -2 * SL : SL; PINS(dlt); }
              if (s == 3) { sl = sl == 2 ? 0 : sl + 1; PINS(sl); }
              if (s == 5) { xa0 += dlt; asm volatile("" : "+v"(xa0)); }
              if (s == 6) { xa1 += dlt; asm volatile("" : "+v"(xa1)); }
              if (s == 9) { wa0 += dlt; asm volatile("" : "+v"(wa0)); }
              if (s == 10) { wa1 += dlt; asm volatile("" : "+v"(wa1)); }
            }
            if (s == 17) rw_(0, 0);
            if (s == 18) rw_(0, 1);
            if (s == 19) rx_(0);
            if (s == 20) rw_(1, 0);
            if (s == 21) rw_(1, 1);
            if (s == 22) rx_(1);
            if (s == 23) rw_(2, 0);
            if (s == 24) rw_(2, 1);
            if (s == 25) rx_(2);
            if (s == 26) rw_(3, 0);
            if (s == 27) rw_(3, 1);
            if (s == 28) rx_(3);
            if (s == 29) rx_(4);
            if (s == 30) rx_(5);
            if (s == 31) rx_(6);
            if (s == 33) rx_(7);
            sbar();
            if (ksc.value == 2) mfma16n(acc[m][n], xv, cat(wf[ks][n][0], wf[ks][n][1]));
            else mfma16(acc[m][n], xv, cat(wf[ks][n][0], wf[ks][n][1]));
          }
        }
      }
      sbar();
    };
    kstep(IC<2>{}, 0);
#pragma unroll 1
    for (int P = 0; P < 7; ++P) { kstep(IC<1>{}, 0); kstep(IC<0>{}, P + 1); }
    if (NM) {
      float t[4 * NM + 1];
      u32 hp[2];
      sfor<0, G0 + 8 * NM + 4>([&](auto sc) {
        constexpr int s = sc.value, m = s >> 2, n = s & 3, e = (s - G0) >> 1, d = e - 1;
        sbar();
        if (s == 1) xf[8] = ld128(xa1 + 8 * 2048);
        if (s == G0) { asm volatile("s_nop 2"); sbar(); }
        if constexpr (s >= G0) {
          constexpr bool he = e < 4 * NM, hd = d >= 0 && d < 4 * NM;
          if constexpr (((s - G0) & 1) == 0) {
            if constexpr (hd) { t[d] = __builtin_fmaf(t[d], -0.6931471805599453f, -0.6931471805599453f); PINV(t[d]); sbar(); }
            if constexpr (he) { t[e] = acc[e >> 2][1][e & 3] * -1.4426950408889634f; PINV(t[e]); sbar(); }
            if constexpr (hd) { t[d] = __builtin_amdgcn_rcpf(t[d]); PINV(t[d]); }
          } else {
            if constexpr (he) { t[e] = __builtin_amdgcn_exp2f(t[e]); PINV(t[e]); sbar(); }
            if constexpr (hd) { t[d] *= acc[d >> 2][0][d & 3]; PINV(t[d]); sbar(); }
            constexpr int c = d - 1;
            if constexpr (c >= 0 && c < 4 * NM && (c & 1)) { hp[(c >> 1) & 1] = pk2(t[c - 1], t[c]); PINV(hp[(c >> 1) & 1]); }
            if constexpr (c >= 4 && c <= 4 * NM && (c & 3) == 0) *LP(u32x2, hg0 + ((c >> 2) - 1) * 32) = u32x2{hp[0], hp[1]};
          }
        }
        sbar();
        if constexpr (s < 36) mfma16(acc[m][n], xf[m], cat(wf[1][n][0], wf[1][n][1]));
      });
      *LP(u32x2, hg0 + (NM - 1) * 32) = u32x2{hp[0], hp[1]};
    }
    sbar();
    __builtin_amdgcn_s_setprio(0);
    STAMP(5);
#pragma unroll
    for (int blk = 0; blk < 2; ++blk) {
      const u32 him = blk ? hg + H1 : hg0;
#pragma unroll
      for (int m = blk ? 0 : NM; m < 9; ++m) {
        const f32x4 a = acc[m][2 * blk], g = acc[m][2 * blk + 1];
        const f32x2 lo = gate2(a.xy, g.xy), hi = gate2(a.zw, g.zw);
        *LP(u32x2, him + m * 32) = u32x2{pk2(lo[0], lo[1]), pk2(hi[0], hi[1])};
      }
      STAMP(6 + 3 * blk);
      sbar();
      if (blk == 0) {
        waitl0();
        bar();
        sbar();
        STAMP(7);
      }
    }
  }
  f32x16 acc2[4];
  bf4 af[8][2];
  u32x4 g[8][4];
  u32 ck[8];
  const u32 key = *LP(const u32, CTL), key4 = 4 * key;
  auto rdaf = [&](int F, int t) {
    const int v = fV(F);
    af[F & 7][t] = ldtr(va[fJ(F)] + (VOFF[v] - (v >= 2 && v <= 4 ? BL : BH)) + fK(F) * 4096 + t * 1024);
  };
  auto gload = [&](auto kc) {
    constexpr int k = kc.value, n = 2 + k / 5, i = k % 5;
    if constexpr (i < 4) gld<(i & 3) * 1024>(g[n][i & 3], goff, gs[n]); else gld1(ck[n], coff, gs[n]);
  };
  auto greload = [&](auto kc) {
    constexpr int k = kc.value, n = 2 + k / 5, i = k % 5;
    if constexpr (i < 4) grl<(i & 3) * 1024>(g[n][i & 3], goff, gs[n]); else grl1(ck[n], coff, gs[n]);
  };
  auto landed = [&](int n) { asm volatile("" : "+v"(g[n][0]), "+v"(g[n][1]), "+v"(g[n][2]), "+v"(g[n][3]), "+v"(ck[n])); };
  u64 bad;
  auto settle = [&](auto nc) {
    constexpr int n = nc.value, F = fC(n) + 6, m = n < 7 && fC(n + 1) < F ? n + 1 : n;
    if (__builtin_expect(bad != 0, 0)) {
#pragma unroll 1
      for (int it = 0; it < (1 << 18); ++it) {
        sfor<5 * (n - 2), 5 * (m - 1)>(greload);
        waitv<0>();
        landed(n); landed(m);
#pragma unroll
        for (int i = 0; i < 4; ++i) ck[n] += mix3(g[n][i]) + g[n][i][3];
        asm volatile("s_nop 1\n\tv_add_u32_dpp %0, %0, %0 row_ror:8 row_mask:0xf bank_mask:0xf\n\ts_nop 1\n\t"
                     "v_add_u32_dpp %0, %0, %0 row_ror:4 row_mask:0xf bank_mask:0xf" : "+v"(ck[n]));
        if (!__any(ck[n] != key4)) break;
      }
      if (m != n) {
#pragma unroll
        for (int i = 0; i < F - fC(m); ++i) ck[m] += mix3(g[m][i]) + g[m][i][3];
      }
      sfor<5 * (m - 1), 30>([&](auto kc) { if constexpr (fL(kc.value) < F) greload(kc); });
    }
  };
#pragma unroll
  for (int F = 0; F < 7; ++F) { rdaf(F, 0); rdaf(F, 1); }
  u32x4 od[2];
  sbar();
  waitl0();
  bar();
  STAMP(10);
  if (HV < 1) vunit<4>(rv, lb, voff0, mem);
  if (HV < 2) vunit<5>(rv, lb, voff0, mem);
  if (HV < 3) vunit<6>(rv, lb, voff0, mem);
  if (SLP) __builtin_amdgcn_s_sleep(SLP);
  sfor<0, 30>([&](auto kc) { if constexpr (fL(kc.value) < 0) gload(kc); });
  sfor<0, 128>([&](auto Fc) {
    constexpr int F = Fc.value;
    constexpr int v = fV(F), j = fJ(F), kk = fK(F);
    sbar();
    if (F == 0) {
#pragma unroll
      for (int i = 0; i < 4; ++i) g[0][i] = __builtin_bit_cast(u32x4, ld128(gla0 + i * (16 * HSTR)));
    }
    if (F == PRE) { STAMP(11); bar(); STAMP(12); }
    if (F == 32) bar();
    if (F == 80) { STAMP(16); bar(); STAMP(17); }
    if (F == 44) STAMP(3);
    if (F == 60) STAMP(4);
    if (F == 72) STAMP(8);
    if (F == 90) STAMP(15);
    if (F == 101) STAMP(21);
    if (F == 104) STAMP(22);
    if (F == 113) STAMP(23);
    constexpr int G1F = PRE > 8 ? PRE : 8;
    if (F >= G1F && F < G1F + 4) g[1][(F - G1F) & 3] = __builtin_bit_cast(u32x4, ld128(gla + H1 + ((F - G1F) & 3) * (16 * HSTR)));
    sfor<2, 8>([&](auto nc) { if constexpr (F == fC(nc.value) + 6) { settle(nc); if (nc.value == 2) STAMP(14); } });
    sbar();
    sfor<0, 30>([&](auto kc) { if constexpr (fL(kc.value) == F) gload(kc); });
    u32 t;
    auto chk = [&](int ph) {
      sfor<2, 8>([&](auto nc) {
        constexpr int n = nc.value, e = F - fC(n);
        if (e == 0 && ph == 0) {
          if (n == 2) STAMP(13);
          waitv<fY(n)>();
          landed(n);
        }
        if (e >= 0 && e < 4) {
          const u32x4 d = g[n][e & 3];
          if (ph == 0) { t = d[0] + d[1] + d[2]; PINV(t); }
          if (ph == 1) { t = __builtin_amdgcn_alignbit(t, t, 13); PINV(t); }
          if (ph == 2) { ck[n] += t + d[3]; PINV(ck[n]); }
        }
        if (e == 4 && ph == 1) DPPADD(ck[n], ck[n], "row_ror:8 row_mask:0xf bank_mask:0xf");
        if (e == 5 && ph == 0) DPPADD(ck[n], ck[n], "row_ror:4 row_mask:0xf bank_mask:0xf");
        if (e == 5 && ph == 2) { bad = __ballot(ck[n] != key4); PINS(bad); }
        sbar();
      });
    };
    chk(0);
    if (F + 7 < 128) rdaf(F + 7, 0);
    sbar();
    chk(1);
    if (F + 7 < 128) {
      const int Fn = F + 7;
      rdaf(Fn, 1);
      sbar();
      if (Fn >= 28 && Fn < 32) { va[Fn & 3] += BL - BH; PINV(va[Fn & 3]); }
      if (Fn >= 76 && Fn < 80) { va[Fn & 3] += BH - BL; PINV(va[Fn & 3]); }
    }
    chk(2);
    if constexpr (F >= 95 && (F - 95) % 12 < 8) {
      constexpr int J = (F - 95) / 12, e = (F - 95) % 12;
#pragma unroll
      for (int i = 0; i < 2; ++i) {
        const int c = (2 * e + i) & 7;
        if (e < 4) { od[c >> 2][c & 3] = pk2(acc2[J][2 * c], acc2[J][2 * c + 1]); PINV(od[c >> 2][c & 3]); }
        else if (e < 6) { auto r = __builtin_amdgcn_permlane16_swap(od[0][c & 3], od[1][c & 3], false, false); od[0][c & 3] = r[0]; od[1][c & 3] = r[1]; }
      }
      if (e >= 6) gsto<J * 64>(ooff[e & 1], od[e & 1], ro);
    }
    sbar();
    if (F < 4) mfma_new(acc2[j], cat(af[F & 7][0], af[F & 7][1]), g[v][kk]);
    else mfma_acc(acc2[j], cat(af[F & 7][0], af[F & 7][1]), g[v][kk]);
  });
  sbar();
  asm volatile("s_nop 7\n\ts_nop 3");
  sbar();
  STAMP(18);
#pragma unroll
  for (int c = 0; c < 8; ++c) od[c >> 2][c & 3] = pk2(acc2[3][2 * c], acc2[3][2 * c + 1]);
#pragma unroll
  for (int c = 0; c < 4; ++c) { auto r = __builtin_amdgcn_permlane16_swap(od[0][c], od[1][c], false, false); od[0][c] = r[0]; od[1][c] = r[1]; }
  gsto<192>(ooff[0], od[0], ro);
  gsto<192>(ooff[1], od[1], ro);
  if (w == 0 && mem == 0 && lane == 0) asm volatile("global_store_dword %0, %1, %2 sc1" ::"v"(32 * CH), "v"(key), "s"(gbase) : "memory");
  STAMP(19);
  STAMP(20);
  STAMP_OUT;
  return;
  }
  {
    __builtin_amdgcn_s_setprio(3);
    u32 lb = u * 1024;
    u32 xo[5];
    xo[0] = (u32)((t0 - 8 + 8 * u + (lane >> 3)) * 1024 + (((lane & 7) ^ ((4 * u + q) & 7)) << 4));
#pragma unroll
    for (int i = 1; i < 5; ++i) xo[i] = xo[0] + i * 32768;
    if (u >= 2) xo[4] = 0x80000000u;
    const u32 swz = (u32)(mem * 256 + ((l15 ^ ((q << 2) | (u & 2))) << 4));
    u32 woff0 = (u32)((4 * u + q) * 2048) + swz;
    u32 voff0 = (u32)((4 * u + q) * 1024) + swz;
    u32 doff = (u32)((lane >> 2) * 1024 + mem * 256 + ((lane >> 1) & 1) * 128 + u * 32 + (lane & 1) * 16);
    u32 x5 = u < 2 ? lb + XO + 16384 : DUMMY, x5s = u < 2 ? SL : 0, xb = batch * 4096000;
    PINS(lb); PINS(x5); PINS(x5s); PINS(xb); PINV(woff0); PINV(voff0); PINV(doff);
#pragma unroll
    for (int i = 0; i < 5; ++i) PINV(xo[i]);
    sbar();
    asm volatile("s_waitcnt lgkmcnt(0)\n\ts_mov_b64 %0, %5\n\ts_mov_b64 %1, %6\n\ts_mov_b64 %2, %7\n\ts_mov_b64 %3, %8\n\ts_mov_b64 %4, %9"
                 : "=&s"(x), "=&s"(w1), "=&s"(dw), "=&s"(w2), "=&s"(ws) : "s"(ka[0]), "s"(ka[1]), "s"(ka[2]), "s"(ka[3]), "s"(kb[0]), "s"(kb));
    STAMP_POST;
    sbar();
    char* gbase = ws + (size_t)grp * GSTR;
    const __amdgpu_buffer_rsrc_t rx = __builtin_amdgcn_make_buffer_rsrc((void*)(x + xb), 0, 4096000, 0x00020000);
    const __amdgpu_buffer_rsrc_t rw = __builtin_amdgcn_make_buffer_rsrc((void*)w1, 0, 1048576, 0x00020000);
    const __amdgpu_buffer_rsrc_t rd = __builtin_amdgcn_make_buffer_rsrc((void*)dw, 0, 15360, 0x00020000);
    const __amdgpu_buffer_rsrc_t rv = __builtin_amdgcn_make_buffer_rsrc((void*)w2, 0, 524288, 0x00020000);
    auto pl = [&](auto Pc, auto ic) {
      constexpr int P = Pc.value, i = ic.value;
      if constexpr (i < 4) glds<XO + (P % 3) * SL + i * 4096, P * 128>(rx, lb, xo[i & 3], 0);
      else if constexpr (i == 4) glds<0, P * 128>(rx, x5 + (P % 3) * x5s, xo[4], 0);
      else {
        constexpr int ks = (i - 5) >> 2, hf = ((i - 5) >> 1) & 1, ab = (i - 5) & 1;
        glds<(P % 3) * SL + ks * 16384 + hf * 4096 + ab * 8192, P * 131072 + ks * 65536 + hf * 32768 + ab * 1024>(rw, lb, woff0, 0);
      }
    };
    auto xunit = [&](auto Pc) { sfor<0, 5>([&](auto ic) { pl(Pc, ic); }); };
    auto wunit = [&](auto Pc, auto kc) { sfor<5 + 4 * kc.value, 9 + 4 * kc.value>([&](auto ic) { pl(Pc, ic); }); };
    char* gdst = gbase + mem * 2 * CH;
    u32x4 T[2][2][8];
    auto punit = [&](auto Pc) {
      constexpr int tb = Pc.value - TB0, blk = (tb >> 1) & 1, hi = tb & 1;
      constexpr bool on = tb >= 0 && tb < 4;
      u32x4 v[8];
      sfor<0, 13>([&](auto ic) {
        constexpr int i = ic.value, c = (i - 5) & 7;
        pl(Pc, ic);
        sbar();
        if (on && i < 4) {
#pragma unroll
          for (int j = 2 * i; j < 2 * i + 2; ++j) {
            const int jj = j ^ (hi << 2);
            u32 r = (u32)(8 * q + jj - (l15 & 7) - 1);
            if ((l15 >> 3) != hi) r = 15;
            v[j & 7] = *LP(const u32x4, DWOFF + lb + (min(r, 15u) << 6) + blk * 32 + hi * 16);
          }
        }
        if (on && i >= 5) {
#pragma unroll
          for (int m = 0; m < 4; ++m)
            T[blk][hi][c][m] = __builtin_amdgcn_perm(v[2 * m + 1][c >> 1], v[2 * m][c >> 1], (c & 1) ? 0x07060302u : 0x05040100u);
          PINV(T[blk][hi][c]);
        }
        sbar();
      });
    };
    STAMP(21);
    punit(IC<0>{});
    STAMP(22);
    glds<DWOFF, 0>(rd, lb, doff, 0);
    xunit(IC<1>{});
    STAMP(20);
    waitv<10>(); bar();
    __builtin_amdgcn_s_setprio(0);
    STAMP(1);
    wunit(IC<1>{}, IC<0>{}); waitv<10>(); bar();
    STAMP(14);
    wunit(IC<1>{}, IC<1>{}); xunit(IC<2>{}); waitv<9>(); bar();
    STAMP(15);
    wunit(IC<2>{}, IC<0>{}); wunit(IC<2>{}, IC<1>{}); waitv<13>(); bar();
    STAMP(16);
    auto pfv = [&]() {
      const u32 po = lane < 32 ? (u32)((gidx * 64 + u * 16 + (lane >> 1)) * 1024 + mem * 256 + (lane & 1) * 128) : 0x80000000u;
      asm volatile("s_mov_b32 m0, %0\n\ts_nop 0\n\tbuffer_load_dword %1, %2, 0 offen lds" ::"s"(DUMMY), "v"(po), "s"(rv) : "memory");
    };
    punit(IC<3>{}); waitv<13>(); bar();
    STAMP(2);
    punit(IC<4>{}); if (PFAT == 4) pfv(); waitv<13 + (PFAT == 4)>(); bar();
    STAMP(17);
    punit(IC<5>{}); if (PFAT == 5) pfv(); waitv<13 + (PFAT == 4 || PFAT == 5)>(); bar();
    STAMP(3);
    punit(IC<6>{}); if (PFAT == 6) pfv(); waitv<13 + (PFAT == 5 || PFAT == 6)>(); bar();
    STAMP(18);
    punit(IC<7>{}); if (PFAT == 7) pfv(); waitv<13 + (PFAT == 6 || PFAT == 7)>(); bar();
    STAMP(4);
    u32 key;
    gld1(key, 32 * CH, gbase);
    vunit<0>(rv, lb, voff0, mem); vunit<1>(rv, lb, voff0, mem);
    waitv<9 + (PFAT == 7)>(); bar();
    STAMP(19);
    vunit<2>(rv, lb, voff0, mem); vunit<3>(rv, lb, voff0, mem);
    if (GPF) {
      const u32 po = u * (8 * CH) + lane * 128, p2 = u * (8 * CH) + 8192 + (lane & 3) * 128;
      asm volatile("s_mov_b32 m0, %0\n\ts_nop 0\n\tglobal_load_lds_dword %1, %3\n\tglobal_load_lds_dword %2, %3" ::"s"(DUMMY), "v"(po), "v"(p2), "s"(gdst) : "memory");
    }
    STAMP(5);
    __builtin_amdgcn_s_setprio(1);
    u32 hr[2][2], hw[2];
    char* gd[2][4];
#pragma unroll
    for (int blk = 0; blk < 2; ++blk) {
      const u32 him = (blk ? H1 : H0) + u * (16 * HSTR);
      const int hoff = ((q == 3 ? 0 : q) + l15) * 16;
      hr[blk][0] = him + hoff; hr[blk][1] = him + (hoff ^ 16); hw[blk] = him + lane * 16;
      PINV(hr[blk][0]); PINV(hr[blk][1]); PINV(hw[blk]);
#pragma unroll
      for (int r = 0; r < 4; ++r) { gd[blk][r] = gdst + (r * 8 + blk) * CH; PINS(gd[blk][r]); }
    }
    u32 l16 = u * 1024 + lane * 16, co = (l15 >> 2) * (8 * CH) + 4096 + u * 64 + q * 16 + (lane & 3) * 4;
    PINV(l16); PINV(co);
    waitv<12 + 2 * GPF>();
    PINV(key);
    ++key;
    if (lane == 0) *LP(u32, CTL) = key;
    waitl0();
#pragma unroll
    for (int blk = 0; blk < 2; ++blk) {
      sbar();
      bar();
      STAMP(6 + 3 * blk);
      bf8 wv[16];
#pragma unroll
      for (int c = 0; c < 8; ++c) {
        wv[c] = ld128(hr[blk][c >> 2] + c * HSTR);
        wv[c + 8] = ld128(hr[blk][c >> 2] + (c + 8) * HSTR);
      }
      sbar();
      f32x4 sv[8];
#pragma unroll
      for (int c = 0; c < 8; ++c) {
        f32x4 d = mfma(__builtin_bit_cast(bf8, T[blk][0][c]), wv[c], f32x4{0, 0, 0, 0});
        sv[c] = mfma(__builtin_bit_cast(bf8, T[blk][1][c]), wv[c + 8], d);
      }
      u32 cs[4];
      auto put = [&](int r) {
        const u32x4 d = u32x4{pk2(sv[0][r], sv[1][r]), pk2(sv[2][r], sv[3][r]), pk2(sv[4][r], sv[5][r]), pk2(sv[6][r], sv[7][r])};
        gst(l16, d, gd[blk][r]);
        *LP(u32x4, hw[blk] + r * 1024) = d;
        cs[r] = mix3(d) + d[3];
        PINV(cs[r]);
      };
      if (CV == 0) {
#pragma unroll
        for (int c = 0; c < 8; ++c) {
          const f32x2 lo = silu2(sv[c].xy), hi = silu2(sv[c].zw);
          sv[c] = f32x4{lo[0], lo[1], hi[0], hi[1]};
        }
#pragma unroll
        for (int r = 0; r < 4; ++r) put(r);
      } else {
#pragma unroll
        for (int c = 0; c < 8; ++c) { sv[c].xy = silu2(sv[c].xy); PINV(sv[c]); }
        sbar();
        put(0);
#pragma unroll
        for (int c = 0; c < 4; ++c) { sv[c].zw = silu2(sv[c].zw); PINV(sv[c]); }
        sbar();
        put(1);
#pragma unroll
        for (int c = 4; c < 8; ++c) { sv[c].zw = silu2(sv[c].zw); PINV(sv[c]); }
        sbar();
        put(2);
        put(3);
      }
      asm volatile("s_nop 1\n\t"
                   "v_add_u32_dpp %0, %0, %0 row_ror:8 row_mask:0xf bank_mask:0x3\n\t"
                   "v_add_u32_dpp %1, %1, %1 row_ror:8 row_mask:0xf bank_mask:0x3\n\t"
                   "v_add_u32_dpp %0, %2, %2 row_ror:8 row_mask:0xf bank_mask:0xc\n\t"
                   "v_add_u32_dpp %1, %3, %3 row_ror:8 row_mask:0xf bank_mask:0xc\n\t"
                   "s_nop 0\n\t"
                   "v_add_u32_dpp %0, %0, %0 row_shl:4 row_mask:0xf bank_mask:0x5\n\t"
                   "v_add_u32_dpp %0, %1, %1 row_shr:4 row_mask:0xf bank_mask:0xa\n\t"
                   "v_sub_u32 %0, %4, %0\n\t"
                   "global_store_dword %5, %0, %6 sc1"
                   : "+v"(cs[0]), "+v"(cs[1]) : "v"(cs[2]), "v"(cs[3]), "v"(key), "v"(co), "s"(gd[blk][0]) : "memory");
      STAMP(7 + 3 * blk);
      if (blk == 0) {
        if (HV > 0) vunit<4>(rv, lb, voff0, mem);
        if (HV > 1) vunit<5>(rv, lb, voff0, mem);
        if (HV > 2) vunit<6>(rv, lb, voff0, mem);
        waitv<13 + 4 * HV + 2 * GPF>();
      } else waitv<10 + 4 * HV>();
      waitl0();
    }
    bar();
    STAMP(12);
    waitv<5>();
    bar();
    vunit<7>(rv, lb, voff0, mem);
    waitv<0>();
    STAMP(13);
    bar();
    STAMP_OUT;
  }
}

extern "C" void kernel_launch(void* const* d_in, const int* in_sizes, int n_in, void* d_out, int out_size, void* d_ws,
                              size_t ws_size, hipStream_t stream) {
  (void)hipFuncSetAttribute((const void*)k_conv_module, hipFuncAttributeMaxDynamicSharedMemorySize, LDSZ);
  k_conv_module<<<256, 512, LDSZ, stream>>>((const char*)d_in[0], (const char*)d_in[1], (const char*)d_in[2], (const char*)d_in[3],
                                            (char*)d_ws, (char*)d_out);
}
```
